```python
import torch

def _softmax(x, axis=-1):
    return torch.softmax(x, dim=axis)

def _gelu(x):
    return torch.nn.functional.gelu(x, approximate='tanh')

def _silu(x):
    return torch.nn.functional.silu(x)

def _sigmoid(x):
    return torch.sigmoid(x)

def _layernorm(x, axis=-1, eps=1e-05):
    m = x.mean(dim=axis, keepdim=True)
    v = x.var(dim=axis, keepdim=True, unbiased=False)
    return (x - m) / torch.sqrt(v + eps)

def _scatter_add(tgt, idx, src, axis=0):
    return tgt.clone().index_add_(axis, idx, src)

def _segment_sum(src, seg, n):
    out = torch.zeros((n,) + src.shape[1:], dtype=src.dtype)
    return out.index_add_(0, seg, src)

def _pad(x, pw):
    flat = tuple((v for pair in reversed(pw) for v in pair))
    return torch.nn.functional.pad(x, flat)
kernel_name = 'adaLN_cond__sliding_block_cached__glu_gated_swiglu__decode'

def setup_inputs():

    _w = lambda a: (lambda t: t.to(torch.bfloat16) if t.is_floating_point() else t)(torch.as_tensor(a))

    def _A_setup():
        B, Sq, D = (8, 1, 448)
        Hq, Hkv, Sk, Dh = (16, 8, 4096, 256)
        assert Hq % Hkv == 0, (Hq, Hkv)
        return {'x': _w((lambda *s: torch.randn(*s, dtype=torch.bfloat16) * 0.1)(B, Sq, D)), 'k_cache': _w((lambda *s: torch.randn(*s, dtype=torch.bfloat16) * 0.8)(B, Hkv, Sk, Dh)), 'v_cache': _w((lambda *s: torch.randn(*s, dtype=torch.bfloat16) * 3.2)(B, Hkv, Sk, Dh)), 'wq': _w((lambda *s: torch.randn(*s, dtype=torch.bfloat16) * 0.8)(D, Hq * Dh) * D ** (-0.5)), 'wo': _w((lambda *s: torch.randn(*s, dtype=torch.bfloat16) * 3.2)(Hq * Dh, D) * (Hq * Dh) ** (-0.5))}
    _A_in = _A_setup()

    def _B_setup():
        B, S, D, F = (8, 1, 448, 1792)
        return {'x': _w((lambda *s: torch.randn(*s, dtype=torch.bfloat16) * 0.1)(B, S, D)), 'w1': _w((lambda *s: torch.randn(*s, dtype=torch.bfloat16) * 3.2)(D, F) * (2.0 / D) ** 0.5), 'w3': _w((lambda *s: torch.randn(*s, dtype=torch.bfloat16) * 3.2)(D, F) * (2.0 / D) ** 0.5), 'w2': _w((lambda *s: torch.randn(*s, dtype=torch.bfloat16) * 3.2)(F, D) * (2.0 / F) ** 0.5)}
    _B_in = _B_setup()
    return {'A_x': _A_in['x'], 'A_k_cache': _A_in['k_cache'], 'A_v_cache': _A_in['v_cache'], 'A_wq': _A_in['wq'], 'A_wo': _A_in['wo'], 'B_x': _B_in['x'], 'B_w1': _B_in['w1'], 'B_w3': _B_in['w3'], 'B_w2': _B_in['w2'], 'c': _w((lambda *s: torch.randn(*s, dtype=torch.bfloat16) * 0.8)(8, 448) * 8.0), 'w_mod': _w((lambda *s: torch.randn(*s, dtype=torch.bfloat16) * 0.8)(448, 6 * 448) * 448 ** (-0.5) * 8.0)}

def reference(A_x, A_k_cache, A_v_cache, A_wq, A_wo, B_x, B_w1, B_w3, B_w2, c, w_mod):

    def _A_ref(x, k_cache, v_cache, wq, wo):
        B, Sq, D = x.shape
        Hq, Dh = (16, 256)
        Hkv, Sk = (k_cache.shape[1], k_cache.shape[2])
        W = min(4096, max(256, Sk // 4))
        rep = Hq // Hkv
        q = (lambda x, *d: x.permute(*d))((x @ wq).reshape(B, Sq, Hq, Dh), 0, 2, 1, 3)
        k = (lambda x, n, axis: torch.repeat_interleave(x, n, dim=axis))(k_cache[:, :, Sk - W:, :], rep, axis=1)
        v = (lambda x, n, axis: torch.repeat_interleave(x, n, dim=axis))(v_cache[:, :, Sk - W:, :], rep, axis=1)
        scores = torch.einsum('bhqd,bhkd->bhqk', q, k) * Dh ** (-0.5)
        p = _softmax(scores, axis=-1)
        o = torch.einsum('bhqk,bhkd->bhqd', p, v)
        return (lambda x, *d: x.permute(*d))(o, 0, 2, 1, 3).reshape(B, Sq, Hq * Dh) @ wo

    def _B_ref(x, w1, w3, w2):
        gate = torch.einsum('bsd,df->bsf', x, w1)
        up = torch.einsum('bsd,df->bsf', x, w3)
        h = _silu(gate) * up
        out = torch.einsum('bsf,fd->bsd', h, w2)
        return out
    x = A_x
    mod = (_silu(c) @ w_mod).reshape(c.shape[0], 6, 1, -1)
    sa, ca, ga, sb, cb, gb = (mod[:, 0].clone(), mod[:, 1].clone(), mod[:, 2].clone(), mod[:, 3].clone(), mod[:, 4].clone(), mod[:, 5].clone())
    h = _layernorm(x, axis=-1) * (1 + ca) + sa
    x = x + ga * _A_ref(h, A_k_cache, A_v_cache, A_wq, A_wo)
    h = _layernorm(x, axis=-1) * (1 + cb) + sb
    x = x + gb * _B_ref(h, B_w1, B_w3, B_w2)
    return x
```

```cpp
#include <hip/hip_runtime.h>
#include <hip/hip_bf16.h>
#include <stdint.h>

typedef unsigned short u16;
__device__ __forceinline__ float bf2f(u16 b) { return __uint_as_float(((unsigned)b) << 16); }
__device__ __forceinline__ u16 f2bf(float f) { unsigned u = __float_as_uint(f); u += 0x7FFFu + ((u >> 16) & 1u); return (u16)(u >> 16); }
__device__ __forceinline__ float rbf(float f) { return bf2f(f2bf(f)); }

constexpr int B = 8, D = 448, HQ = 16, HKV = 8, SK = 4096, DH = 256, W = 1024, F = 1792;

__global__ void __launch_bounds__(256) k_mod(const u16* c, const u16* wmod, float* mod) {
  __shared__ float s[B][D];
  __shared__ float red[4][B][64];
  int tid = threadIdx.x;
  for (int i = tid; i < B * D; i += 256) { float v = bf2f(c[i]); s[i / D][i % D] = rbf(v / (1.f + __expf(-v))); }
  __syncthreads();
  int col = blockIdx.x * 64 + (tid & 63), kg = tid >> 6;
  float acc[B] = {};
  for (int k = kg * 112; k < kg * 112 + 112; ++k) { float w = bf2f(wmod[(size_t)k * 2688 + col]); for (int b = 0; b < B; ++b) acc[b] += s[b][k] * w; }
  for (int b = 0; b < B; ++b) red[kg][b][tid & 63] = acc[b];
  __syncthreads();
  for (int i = tid; i < B * 64; i += 256) { int b = i / 64, cc = i % 64; mod[b * 2688 + blockIdx.x * 64 + cc] = rbf(red[0][b][cc] + red[1][b][cc] + red[2][b][cc] + red[3][b][cc]); }
}

__device__ void ln_mod(float (*xs)[D], const float* mod, int so, int co, int tid) {
  int lane = tid & 63, wv = tid >> 6;
  for (int b = wv; b < B; b += 4) {
    float sum = 0; for (int i = lane; i < D; i += 64) sum += xs[b][i];
    for (int m = 32; m >= 1; m >>= 1) sum += __shfl_xor(sum, m);
    float mean = sum / D;
    float sq = 0; for (int i = lane; i < D; i += 64) { float d = xs[b][i] - mean; sq += d * d; }
    for (int m = 32; m >= 1; m >>= 1) sq += __shfl_xor(sq, m);
    float var = rbf(sq / D); float mb = rbf(mean);
    float den = rbf(sqrtf(rbf(var + 1e-5f)));
    for (int i = lane; i < D; i += 64) {
      float ln = rbf(rbf(xs[b][i] - mb) / den);
      float t = rbf(ln * rbf(1.f + mod[b * 2688 + co + i]));
      xs[b][i] = rbf(t + mod[b * 2688 + so + i]);
    }
  }
}

__global__ void __launch_bounds__(256) k_q(const u16* x, const float* mod, const u16* wq, float* q) {
  __shared__ float s[B][D];
  __shared__ float red[4][B][64];
  int tid = threadIdx.x;
  for (int i = tid; i < B * D; i += 256) s[i / D][i % D] = bf2f(x[i]);
  __syncthreads();
  ln_mod(s, mod, 0, D, tid);
  __syncthreads();
  int col = blockIdx.x * 64 + (tid & 63), kg = tid >> 6;
  float acc[B] = {};
  for (int k = kg * 112; k < kg * 112 + 112; ++k) { float w = bf2f(wq[(size_t)k * 4096 + col]); for (int b = 0; b < B; ++b) acc[b] += s[b][k] * w; }
  for (int b = 0; b < B; ++b) red[kg][b][tid & 63] = acc[b];
  __syncthreads();
  for (int i = tid; i < B * 64; i += 256) { int b = i / 64, cc = i % 64; q[b * 4096 + blockIdx.x * 64 + cc] = rbf(red[0][b][cc] + red[1][b][cc] + red[2][b][cc] + red[3][b][cc]); }
}

__global__ void __launch_bounds__(256) k_scores(const float* q, const u16* kc, float* sc) {
  int blk = blockIdx.x, sp = blk & 3, bh = blk >> 2, b = bh >> 3, hk = bh & 7;
  int tid = threadIdx.x, lane = tid & 63, wv = tid >> 6;
  int half = lane >> 5, ch = lane & 31;
  float q0[8], q1[8];
  for (int j = 0; j < 8; ++j) { q0[j] = q[b * 4096 + (2 * hk) * 256 + ch * 8 + j]; q1[j] = q[b * 4096 + (2 * hk + 1) * 256 + ch * 8 + j]; }
  const u16* kb = kc + ((size_t)bh * SK + (SK - W) + sp * 256) * DH;
  for (int it = 0; it < 32; ++it) {
    int key = wv * 64 + it * 2 + half;
    uint4 v = *(const uint4*)(kb + (size_t)key * DH + ch * 8);
    unsigned w[4] = {v.x, v.y, v.z, v.w};
    float a0 = 0, a1 = 0;
    for (int j = 0; j < 4; ++j) { float lo = __uint_as_float(w[j] << 16), hi = __uint_as_float(w[j] & 0xffff0000u); a0 += lo * q0[2 * j] + hi * q0[2 * j + 1]; a1 += lo * q1[2 * j] + hi * q1[2 * j + 1]; }
    for (int m = 16; m >= 1; m >>= 1) { a0 += __shfl_xor(a0, m); a1 += __shfl_xor(a1, m); }
    if (ch == 0) { sc[(b * 16 + 2 * hk) * W + sp * 256 + key] = rbf(a0) * 0.0625f; sc[(b * 16 + 2 * hk + 1) * W + sp * 256 + key] = rbf(a1) * 0.0625f; }
  }
}

__global__ void __launch_bounds__(256) k_pv(const float* sc, const u16* vc, float* o) {
  __shared__ float p[2][W];
  __shared__ float red[32][2][64];
  __shared__ float st[2][4];
  int blk = blockIdx.x, sp = blk & 3, bh = blk >> 2, b = bh >> 3, hk = bh & 7;
  int tid = threadIdx.x, lane = tid & 63, wv = tid >> 6;
  float s0[4], s1[4], m0 = -INFINITY, m1 = -INFINITY;
  for (int j = 0; j < 4; ++j) { s0[j] = sc[(b * 16 + 2 * hk) * W + tid + 256 * j]; s1[j] = sc[(b * 16 + 2 * hk + 1) * W + tid + 256 * j]; m0 = fmaxf(m0, s0[j]); m1 = fmaxf(m1, s1[j]); }
  for (int m = 32; m >= 1; m >>= 1) { m0 = fmaxf(m0, __shfl_xor(m0, m)); m1 = fmaxf(m1, __shfl_xor(m1, m)); }
  if (lane == 0) { st[0][wv] = m0; st[1][wv] = m1; }
  __syncthreads();
  m0 = fmaxf(fmaxf(st[0][0], st[0][1]), fmaxf(st[0][2], st[0][3])); m1 = fmaxf(fmaxf(st[1][0], st[1][1]), fmaxf(st[1][2], st[1][3]));
  __syncthreads();
  float l0 = 0, l1 = 0;
  for (int j = 0; j < 4; ++j) { s0[j] = __expf(s0[j] - m0); s1[j] = __expf(s1[j] - m1); l0 += s0[j]; l1 += s1[j]; }
  for (int m = 32; m >= 1; m >>= 1) { l0 += __shfl_xor(l0, m); l1 += __shfl_xor(l1, m); }
  if (lane == 0) { st[0][wv] = l0; st[1][wv] = l1; }
  __syncthreads();
  l0 = st[0][0] + st[0][1] + st[0][2] + st[0][3]; l1 = st[1][0] + st[1][1] + st[1][2] + st[1][3];
  for (int j = 0; j < 4; ++j) { p[0][tid + 256 * j] = rbf(s0[j] / l0); p[1][tid + 256 * j] = rbf(s1[j] / l1); }
  __syncthreads();
  int kr = tid >> 3, ch = tid & 7;
  const u16* vb = vc + ((size_t)bh * SK + (SK - W)) * DH + sp * 64 + ch * 8;
  float a0[8] = {}, a1[8] = {};
  for (int it = 0; it < 32; ++it) {
    int key = it * 32 + kr;
    uint4 v = *(const uint4*)(vb + (size_t)key * DH);
    unsigned w[4] = {v.x, v.y, v.z, v.w};
    float p0 = p[0][key], p1 = p[1][key];
    for (int j = 0; j < 4; ++j) { float lo = __uint_as_float(w[j] << 16), hi = __uint_as_float(w[j] & 0xffff0000u); a0[2 * j] += p0 * lo; a0[2 * j + 1] += p0 * hi; a1[2 * j] += p1 * lo; a1[2 * j + 1] += p1 * hi; }
  }
  for (int j = 0; j < 8; ++j) { red[kr][0][ch * 8 + j] = a0[j]; red[kr][1][ch * 8 + j] = a1[j]; }
  __syncthreads();
  if (tid < 128) { int h = tid >> 6, d = tid & 63; float s = 0; for (int i = 0; i < 32; ++i) s += red[i][h][d]; o[b * 4096 + (2 * hk + h) * 256 + sp * 64 + d] = rbf(s); }
}

template <int K, int N, int KSL>
__global__ void __launch_bounds__(256) k_part(const float* act, const u16* w, float* part) {
  __shared__ float red[4][B][64];
  int tid = threadIdx.x; int ns = blockIdx.x, ks = blockIdx.y;
  int col = ns * 64 + (tid & 63), kg = tid >> 6;
  float acc[B] = {};
  for (int k = ks * KSL + kg * (KSL / 4); k < ks * KSL + (kg + 1) * (KSL / 4); ++k) { float wv = bf2f(w[(size_t)k * N + col]); for (int b = 0; b < B; ++b) acc[b] += act[b * K + k] * wv; }
  for (int b = 0; b < B; ++b) red[kg][b][tid & 63] = acc[b];
  __syncthreads();
  for (int i = tid; i < B * 64; i += 256) { int b = i / 64, cc = i % 64; part[(ks * B + b) * N + ns * 64 + cc] = red[0][b][cc] + red[1][b][cc] + red[2][b][cc] + red[3][b][cc]; }
}

__global__ void __launch_bounds__(256) k_ffn1(const u16* x, const float* mod, const float* part, const u16* w1, const u16* w3, float* hh, float* x1out) {
  __shared__ float s[B][D];
  __shared__ float red[2][4][B][64];
  int tid = threadIdx.x;
  for (int i = tid; i < B * D; i += 256) {
    float a = 0; for (int ks = 0; ks < 32; ++ks) a += part[ks * B * D + i];
    int b = i / D, d = i % D;
    float x1 = rbf(bf2f(x[i]) + rbf(mod[b * 2688 + 2 * D + d] * rbf(a)));
    s[b][d] = x1; if (blockIdx.x == 0) x1out[i] = x1;
  }
  __syncthreads();
  ln_mod(s, mod, 3 * D, 4 * D, tid);
  __syncthreads();
  int col = blockIdx.x * 64 + (tid & 63), kg = tid >> 6;
  float g[B] = {}, u[B] = {};
  for (int k = kg * 112; k < kg * 112 + 112; ++k) { float a = bf2f(w1[(size_t)k * F + col]), c = bf2f(w3[(size_t)k * F + col]); for (int b = 0; b < B; ++b) { g[b] += s[b][k] * a; u[b] += s[b][k] * c; } }
  for (int b = 0; b < B; ++b) { red[0][kg][b][tid & 63] = g[b]; red[1][kg][b][tid & 63] = u[b]; }
  __syncthreads();
  for (int i = tid; i < B * 64; i += 256) { int b = i / 64, cc = i % 64;
    float gg = rbf(red[0][0][b][cc] + red[0][1][b][cc] + red[0][2][b][cc] + red[0][3][b][cc]);
    float uu = rbf(red[1][0][b][cc] + red[1][1][b][cc] + red[1][2][b][cc] + red[1][3][b][cc]);
    float sg = rbf(gg / (1.f + __expf(-gg)));
    hh[b * F + blockIdx.x * 64 + cc] = rbf(sg * uu); }
}

__global__ void __launch_bounds__(256) k_final(const float* x1, const float* mod, const float* part, u16* out) {
  int i = blockIdx.x * 256 + threadIdx.x;
  if (i >= B * D) return;
  float a = 0; for (int ks = 0; ks < 28; ++ks) a += part[ks * B * D + i];
  int b = i / D, d = i % D;
  out[i] = f2bf(x1[i] + rbf(mod[b * 2688 + 5 * D + d] * rbf(a)));
}

extern "C" void kernel_launch(void* const* d_in, const int* in_sizes, int n_in, void* d_out, int out_size, void* d_ws, size_t ws_size, hipStream_t stream) {
  const u16* x = (const u16*)d_in[0]; const u16* kc = (const u16*)d_in[1]; const u16* vc = (const u16*)d_in[2];
  const u16* wq = (const u16*)d_in[3]; const u16* wo = (const u16*)d_in[4];
  const u16* w1 = (const u16*)d_in[6]; const u16* w3 = (const u16*)d_in[7]; const u16* w2 = (const u16*)d_in[8];
  const u16* c = (const u16*)d_in[9]; const u16* wmod = (const u16*)d_in[10];
  float* ws = (float*)d_ws;
  float* mod = ws; float* q = mod + 32768; float* sc = q + 32768; float* o = sc + 8 * 16 * 1024; float* p1 = o + 32768; float* hh = p1 + 32 * 8 * 448; float* x1 = hh + 8 * 1792; float* p2 = x1 + 4096;
  k_mod<<<42, 256, 0, stream>>>(c, wmod, mod);
  k_q<<<64, 256, 0, stream>>>(x, mod, wq, q);
  k_scores<<<256, 256, 0, stream>>>(q, kc, sc);
  k_pv<<<256, 256, 0, stream>>>(sc, vc, o);
  k_part<4096, 448, 128><<<dim3(7, 32), 256, 0, stream>>>(o, wo, p1);
  k_ffn1<<<28, 256, 0, stream>>>(x, mod, p1, w1, w3, hh, x1);
  k_part<1792, 448, 64><<<dim3(7, 28), 256, 0, stream>>>(hh, w2, p2);
  k_final<<<14, 256, 0, stream>>>(x1, mod, p2, (u16*)d_out);
}
```

```cpp
#include <hip/hip_runtime.h>
#include <stdint.h>

#define GAS __attribute__((address_space(1)))
#define LAS __attribute__((address_space(3)))
typedef unsigned v4u __attribute__((ext_vector_type(4)));
typedef unsigned v2u __attribute__((ext_vector_type(2)));
typedef float f4 __attribute__((ext_vector_type(4)));
typedef float f2 __attribute__((ext_vector_type(2)));
typedef __bf16 bf8 __attribute__((ext_vector_type(8)));
typedef __bf16 bf2 __attribute__((ext_vector_type(2)));
typedef short s4 __attribute__((ext_vector_type(4)));
typedef unsigned long long u64;
typedef __amdgpu_buffer_rsrc_t rsrc_t;

#define STAMPS 0

namespace {
constexpr int D = 448;
constexpr int WS_EPOCH = 0;
constexpr int WS_MOD = 4096;
constexpr int WS_Q = WS_MOD + 8 * 672 * 16;
constexpr int WS_ATT = WS_Q + 8 * 1024 * 16;
constexpr int WS_S1P = WS_ATT + 16 * 8 * 4 * 64 * 16;
constexpr int WS_X1 = WS_S1P + 8 * 7 * 8 * 16 * 16;
constexpr int WS_S3P = WS_X1 + 8 * 112 * 16;
constexpr int WS_END = WS_S3P + 8 * 7 * 28 * 16 * 16;
constexpr int WS_TS = 4 << 20;
constexpr int L_TILE = 0;
constexpr int L_TILB = 57344;
constexpr int L_PART = L_TILB + 65536;
constexpr int L_ACT = L_PART + 10 * 2048;
constexpr int AS = 1056;
constexpr int L_HH = L_ACT + 8 * AS;
constexpr int HS = 160;
constexpr int L_Q = L_HH + 8 * HS;
constexpr int L_P = L_Q + 1024;
constexpr int L_STAT = L_P + 10 * 128;
constexpr int L_FLAG = L_STAT + 10 * 16;
constexpr int L_DEAD = L_FLAG + 512;
constexpr int L_END = L_DEAD + 2048;
constexpr int F_SM = 0, F_PV = 16, F_Q = 32, F_ISS = 48, F_TILE = 64, F_ACT = 80, F_GO = 96;
#define DLY_P 5
#define DLY_KV 18
#define NUN 0xAA57
#define ABL 0
#define PFN 6
constexpr int CODE_KB = STAMPS ? 18 : 17;
constexpr int SPIN_G = 1 << 20, SPIN_L = 1 << 22;

__device__ __forceinline__ rsrc_t mk(const void* p, int n) { return __builtin_amdgcn_make_buffer_rsrc((void*)p, 0, n, 0x00020000); }
__device__ __forceinline__ float asf(unsigned u) { return __builtin_bit_cast(float, u); }
__device__ __forceinline__ unsigned asu(float f) { return __builtin_bit_cast(unsigned, f); }
__device__ __forceinline__ unsigned pk(float lo, float hi) { bf2 v = {(__bf16)lo, (__bf16)hi}; return __builtin_bit_cast(unsigned, v); }
__device__ __forceinline__ float rbf(float f) { return (float)(__bf16)f; }
__device__ __forceinline__ float blo(unsigned u) { return asf(u << 16); }
__device__ __forceinline__ float bhi(unsigned u) { return asf(u & 0xffff0000u); }
__device__ __forceinline__ f4 mfma(v4u a, v4u b, f4 c) {
  return __builtin_amdgcn_mfma_f32_16x16x32_bf16(__builtin_bit_cast(bf8, a), __builtin_bit_cast(bf8, b), c, 0, 0, 0);
}
template <int CTRL> __device__ __forceinline__ float dpp(float x) { return asf(__builtin_amdgcn_mov_dpp(asu(x), CTRL, 0xf, 0xf, true)); }
constexpr int XOR1 = 0xB1, XOR2 = 0x4E, ROR4 = 0x124, ROR8 = 0x128;
__device__ __forceinline__ float rows_sum(float x) {
  auto s = __builtin_amdgcn_permlane16_swap(asu(x), asu(x), false, false);
  x = asf(s[0]) + asf(s[1]);
  auto t = __builtin_amdgcn_permlane32_swap(asu(x), asu(x), false, false);
  return asf(t[0]) + asf(t[1]);
}
__device__ __forceinline__ float rows_max(float x) {
  auto s = __builtin_amdgcn_permlane16_swap(asu(x), asu(x), false, false);
  x = fmaxf(asf(s[0]), asf(s[1]));
  auto t = __builtin_amdgcn_permlane32_swap(asu(x), asu(x), false, false);
  return fmaxf(asf(t[0]), asf(t[1]));
}
__device__ __forceinline__ float wave_sum(float x) {
  x += dpp<XOR1>(x); x += dpp<XOR2>(x); x += dpp<ROR4>(x); x += dpp<ROR8>(x);
  return rows_sum(x);
}
#define CBAR() asm volatile("" ::: "memory")
__device__ __forceinline__ u64 rt() { u64 t; __builtin_amdgcn_sched_barrier(0); asm volatile("s_memrealtime %0\n\ts_waitcnt lgkmcnt(0)" : "=s"(t) :: "memory"); __builtin_amdgcn_sched_barrier(0); return t; }
#define STAMP(i)

__device__ __forceinline__ v4u enc4(f4 v, unsigned tag) {
  v4u r;
  r.x = __builtin_amdgcn_perm(tag, asu(v.x) + 0x80u, 0x03020104u);
  r.y = __builtin_amdgcn_perm(tag, asu(v.y) + 0x80u, 0x03020105u);
  r.z = __builtin_amdgcn_perm(tag, asu(v.z) + 0x80u, 0x03020106u);
  r.w = __builtin_amdgcn_perm(tag, asu(v.w) + 0x80u, 0x03020107u);
  return r;
}
__device__ __forceinline__ v4u enc4b(f4 v, unsigned tag) {
  return v4u{__builtin_amdgcn_perm(tag, asu(v.x), 0x03020104u), __builtin_amdgcn_perm(tag, asu(v.y), 0x03020105u),
             __builtin_amdgcn_perm(tag, asu(v.z), 0x03020106u), __builtin_amdgcn_perm(tag, asu(v.w), 0x03020107u)};
}
__device__ __forceinline__ unsigned tag4(v4u g) {
  return __builtin_amdgcn_perm(g.y, g.x, 0x0c0c0400u) | __builtin_amdgcn_perm(g.w, g.z, 0x04000c0cu);
}
__device__ __forceinline__ f4 dec4(v4u g) { return f4{asf(g.x & ~255u), asf(g.y & ~255u), asf(g.z & ~255u), asf(g.w & ~255u)}; }
__device__ __forceinline__ void put(rsrc_t ws, int off, v4u g) { __builtin_amdgcn_raw_buffer_store_b128(g, ws, off, 0, 16); }
__device__ __forceinline__ unsigned tagA(v4u g) { return __builtin_amdgcn_perm(g.w, g.y, 0x05040100u); }
__device__ __forceinline__ float lseA(v4u g) { return asf(__builtin_amdgcn_perm(g.y, g.w, 0x07060302u)); }

template <int N, int STEP, bool ATT>
__device__ __forceinline__ void poll(v4u (&x)[N], rsrc_t ws, int off, unsigned tag) {
#pragma nounroll
  for (int it = 0; it < SPIN_G; ++it) {
    CBAR();
#pragma unroll
    for (int l = 0; l < N; ++l) x[l] = __builtin_amdgcn_raw_buffer_load_b128(ws, off + l * STEP, 0, 16);
    unsigned bad = 0;
#pragma unroll
    for (int l = 0; l < N; ++l) bad |= (ATT ? tagA(x[l]) : tag4(x[l])) ^ tag;
    if (__builtin_amdgcn_ballot_w64(bad != 0) == 0) break;
  }
}
__device__ __forceinline__ int lseen(unsigned v) { return __builtin_popcount((unsigned)__builtin_amdgcn_ballot_w64(v != 0u) & 0xffffu); }
__device__ __forceinline__ void lwait(volatile LAS unsigned* f, int want) {
  volatile LAS unsigned* p = f + (threadIdx.x & 15);
#pragma nounroll
  for (int it = 0; it < SPIN_L; ++it) {
    if (lseen(*p) >= want) break;
    __builtin_amdgcn_s_sleep(1);
  }
  CBAR();
}
__device__ __forceinline__ void lset(volatile LAS unsigned* f, int w) { CBAR(); f[w] = 1u; }
template <bool NT>
__device__ __forceinline__ void dma(rsrc_t rs, int voff, int soff, unsigned ldsaddr) {
  unsigned keep;
  if (NT)
    asm volatile("s_mov_b32 %0, m0\n\ts_mov_b32 m0, %3\n\ts_nop 0\n\tbuffer_load_dwordx4 %1, %2, %4 offen nt lds\n\ts_mov_b32 m0, %0"
                 : "=&s"(keep) : "v"(voff), "s"(rs), "s"(ldsaddr), "s"(soff) : "memory");
  else
    asm volatile("s_mov_b32 %0, m0\n\ts_mov_b32 m0, %3\n\ts_nop 0\n\tbuffer_load_dwordx4 %1, %2, %4 offen lds\n\ts_mov_b32 m0, %0"
                 : "=&s"(keep) : "v"(voff), "s"(rs), "s"(ldsaddr), "s"(soff) : "memory");
}
template <bool NT>
__device__ __forceinline__ void dma_tile(rsrc_t rs, int base, int stride, int q0, int dq, int n, unsigned lds, int dl, int lane) {
  const int rowoff = (lane >> 3) * stride;
  const int v0 = rowoff + (((lane & 7) ^ (((lane >> 4) & 1) << 1)) << 4), v1 = v0 ^ 64;
  int so = base + q0 * 8 * stride;
  unsigned la = lds;
#pragma nounroll
  for (int i = 0; i < n; ++i) {
    dma<NT>(rs, v0, so, la);
    dma<NT>(rs, v1, so + 8 * stride, la + 1024);
    so += dq * 8 * stride; la += dl;
  }
}
__device__ __forceinline__ v4u trfrag(LAS char* p) {
  s4 a = __builtin_amdgcn_ds_read_tr16_b64_v4i16((LAS s4*)p), b = __builtin_amdgcn_ds_read_tr16_b64_v4i16((LAS s4*)(p + 512));
  v2u x = __builtin_bit_cast(v2u, a), y = __builtin_bit_cast(v2u, b);
  return v4u{x.x, x.y, y.x, y.y};
}
__device__ __forceinline__ int tr_base(int lane, int nt) {
  const int g = lane >> 4, q4 = (lane & 15) >> 2, p = lane & 3;
  return (8 * g + q4) * 128 + ((nt ^ ((q4 >> 1) | ((g & 1) << 1))) << 5) + p * 8;
}
__device__ __forceinline__ void ln_norm(float (&x)[8], bool act) {
  float s = 0.f;
#pragma unroll
  for (int i = 0; i < 8; ++i) s += x[i];
  const float mean = wave_sum(act ? s : 0.f) * (1.0f / D);
  float q = 0.f;
#pragma unroll
  for (int i = 0; i < 8; ++i) { const float d = x[i] - mean; q += d * d; }
  const float var = rbf(wave_sum(act ? q : 0.f) * (1.0f / D));
  const float mb = rbf(mean);
  const float inv = __builtin_amdgcn_rcpf(rbf(__builtin_amdgcn_sqrtf(rbf(var + 1e-5f))));
#pragma unroll
  for (int i = 0; i < 8; ++i) x[i] = rbf(rbf(x[i] - mb) * inv);
}
__device__ __forceinline__ void mod_store(const float (&ln)[8], const v4u (&m)[4], LAS char* row, int L) {
  float h[8];
#pragma unroll
  for (int i = 0; i < 8; ++i) {
    const float sh = bhi(m[i >> 2][i & 3]), sc = bhi(m[2 + (i >> 2)][i & 3]);
    h[i] = rbf(ln[i] * rbf(1.0f + sc)) + sh;
  }
  *(LAS v2u*)(row + L * 8) = v2u{pk(h[0], h[1]), pk(h[2], h[3])};
  *(LAS v2u*)(row + 448 + L * 8) = v2u{pk(h[4], h[5]), pk(h[6], h[7])};
}
__device__ __forceinline__ float silu(float v) {
  const float d = 1.0f + __builtin_amdgcn_exp2f(fminf(-1.4426950408889634f * v, 126.0f)), r = __builtin_amdgcn_rcpf(d), q = v * r;
  return __builtin_fmaf(__builtin_fmaf(-q, d, v), r, q);
}
#define PIN(x) asm volatile("" : "+v"(x))
#define BARRIER() asm volatile("s_waitcnt lgkmcnt(0)\n\ts_barrier" ::: "memory")
}

typedef unsigned s16u __attribute__((ext_vector_type(16)));
typedef unsigned s8u __attribute__((ext_vector_type(8)));
#define KPTR(A, i) ((char*)(((u64)(A)[2 * (i) + 1] << 32) | (A)[2 * (i)]))
#define kc KPTR(KA, 0)
#define vc KPTR(KA, 1)
#define ws_ KPTR(KA, 2)
#define wmod KPTR(KA, 3)
#define wq KPTR(KA, 4)
#define cin KPTR(KA, 5)
#define xin KPTR(KA, 6)
#define wo KPTR(KA, 7)
#define w1 KPTR(KB, 0)
#define w3 KPTR(KB, 1)
#define w2 KPTR(KB, 2)
#define out KPTR(KB, 3)
#define KWAIT() do { asm volatile("s_waitcnt lgkmcnt(0) ; KWAIT" : "={s[72:87]}"(KA), "={s[88:95]}"(KB)); ws = mk(ws_, WS_END); \
    int zoff = WS_EPOCH; PIN(zoff); epoch = __builtin_amdgcn_raw_buffer_load_b32(ws, zoff, 0, 16); } while (0)
#define tag (epoch + 0x01010101u)

__global__ void __launch_bounds__(768, 3)
fused_decode(const char*, const char*, char*, const char*, const char*, const char*, const char*, const char*, const char*, const char*, const char*, char*) {
  __shared__ __attribute__((aligned(16))) char smem[L_END];
  LAS char* S = (LAS char*)smem;
  volatile LAS unsigned* FL = (volatile LAS unsigned*)(S + L_FLAG);
  STAMP(0);
  const int blk = blockIdx.x, lane = threadIdx.x & 63, w = __builtin_amdgcn_readfirstlane(threadIdx.x >> 6);
  const unsigned lb = (unsigned)(size_t)S;
  const int x = blk & 7, r = blk >> 3, b = r >> 2, j = r & 3;
  s16u KA; s8u KB;
  {
    u64 pc; unsigned keep;
    const v2u zero = {0u, 0u};
    asm volatile(
        "s_cmp_lg_u32 %[w], 0\n\ts_cbranch_scc1 1f\n\t"
        "ds_write_b64 %[fa], %[z]\n\t"
        "s_getpc_b64 %[pc]\n\ts_mov_b32 %[k], m0\n\ts_mov_b32 m0, %[dead]\n\ts_nop 0\n\t"
        "global_load_lds_dwordx4 %[co], %[pc]\n\ts_mov_b32 m0, %[k]\n\t"
        "s_waitcnt lgkmcnt(0)\n"
        "1:\n\t"
        "s_load_dwordx16 s[72:87], %[ka], 0\n\ts_load_dwordx8 s[88:95], %[ka], 64\n\ts_barrier"
        : [pc] "=&s"(pc), [k] "=&s"(keep)
        : [w] "s"(w), [fa] "v"(lb + L_FLAG + lane * 8), [z] "v"(zero), [dead] "s"(lb + L_DEAD), [co] "v"((r < CODE_KB ? CODE_KB - 1 - r : 31 - r) * 1024 + 384 + lane * 16),
          [ka] "s"(__builtin_amdgcn_kernarg_segment_ptr())
        : "scc", "memory", "s72", "s73", "s74", "s75", "s76", "s77", "s78", "s79", "s80", "s81", "s82", "s83", "s84", "s85", "s86", "s87",
          "s88", "s89", "s90", "s91", "s92", "s93", "s94", "s95");
  }
  constexpr int N0 = NUN & 15, N1 = (NUN >> 4) & 15, N2 = (NUN >> 8) & 15;
  static_assert(N0 + N1 + N2 + (NUN >> 12) == 32 && N0 <= 8 && N1 <= 7 && N1 >= 4, "unit split");
  const int nun = (NUN >> (4 * j)) & 15, u0 = ((N0 << 8 | (N0 + N1) << 16 | (N0 + N1 + N2) << 24) >> (8 * j)) & 255, nh = 12 - nun;
  const int idx = b * 8 + x;
  const bool urgent = x < 2 && b < 7;
  const int k0 = x < 2 ? 48 + x : (x - 2) * 8 + b;
  const bool isP0 = j == 0 && (urgent || k0 < 14), isP1 = j == 1, isS1 = isP1 && b < 7;
  const bool isF = j == 0 && !urgent && k0 >= 14 && k0 < 42;
  const int fidx = k0 - 14;
  const int g = lane >> 4, n = lane & 15;
  rsrc_t ws;
  unsigned epoch;
  __builtin_amdgcn_s_setprio(3);
  const bool act = lane < 56;
  const int L = act ? lane : 55;
  const bool isP = isP0 || isP1, pf = j >= 2 && urgent && w < 2;
  const int tile = isP1 ? idx : isF ? fidx : urgent ? 2 * b + x : 14 + 2 * k0;
  KWAIT();
  if (isP && w >= 4) dma<false>(mk(isP0 ? cin : xin, 8 * D * 2), lane * 16, (w - 4) * 896, lb + L_PART + (w - 4) * 1024);
  if (!(urgent && j != 1)) __builtin_amdgcn_s_sleep(DLY_P);
  if (((isP || isF) && w < 4) || pf) {
    const int sa = isP1 ? 8192 : isF ? 3584 : 5376;
    dma_tile<true>(mk(isP1 ? wq : isF ? w1 : wmod, D * sa), tile * 128, sa, pf ? 56 - PFN * (2 * j + w - 3) : w * 2, pf ? 2 : 8, pf ? PFN / 2 : 7,
                   lb + (pf ? L_DEAD : L_TILE + w * 2048), pf ? 0 : 8192, lane);
    if (isS1 || (j == 0 && !urgent))
      dma_tile<true>(mk(isP1 ? wo : isF ? w3 : wmod, isP1 ? 4096 * 896 : D * sa), isP1 ? x * 512 * 896 + b * 128 : isF ? tile * 128 : tile * 128 + 128, isP1 ? 896 : sa,
                     w * 2, 8, isP1 ? 8 : 7, lb + L_TILB + w * 2048, 8192, lane);
    if (isF) {
      asm volatile("s_waitcnt vmcnt(0)" ::: "memory");
      lset(FL + F_TILE, w);
      STAMP(1);
    }
  }
  if (__builtin_expect(isP, 0)) {
    if (w < 4) {
      unsigned tb = L_TILE + tr_base(lane, w), ao = L_ACT + (n & 7) * AS + g * 16;
      int off = n >= 8 ? 0x7ffff000 : (isP0 ? WS_MOD + n * 672 * 16 : WS_Q + n * 1024 * 16) + (tile * 16 + w * 4 + g) * 16;
      PIN(tb); PIN(ao); PIN(off);
      v4u wf[14], bq[14];
      if (isP0) {
        lwait(FL + F_ACT, 8);
#pragma unroll
        for (int ks = 0; ks < 14; ++ks) bq[ks] = *(LAS v4u*)(S + ao + ks * 64);
      }
      asm volatile("s_waitcnt vmcnt(0)" ::: "memory");
      lset(FL + F_TILE, w);
      STAMP(1);
      lwait(FL + F_TILE, 4);
#pragma nounroll
      for (int t = (isP1 || urgent) ? 1 : 0; t < 2; ++t) {
#pragma unroll
        for (int ks = 0; ks < 14; ++ks) wf[ks] = trfrag(S + tb + ks * 4096);
        if (!isP0) {
          lwait(FL + F_ACT, 8);
#pragma unroll
          for (int ks = 0; ks < 14; ++ks) bq[ks] = *(LAS v4u*)(S + ao + ks * 64);
        }
        __builtin_amdgcn_sched_barrier(0);
        f4 acc = {0.f, 0.f, 0.f, 0.f};
#pragma unroll
        for (int ks = 0; ks < 14; ++ks) acc = mfma(wf[ks], bq[ks], acc);
        put(ws, off, enc4b(f4{rbf(acc.x), rbf(acc.y), rbf(acc.z), rbf(acc.w)}, tag));
        tb += L_TILB; off += 256;
      }
      lset(FL + F_GO, w);
      STAMP(7);
    } else {
      const int row = w - 4;
      asm volatile("s_waitcnt vmcnt(0)" ::: "memory");
      const v2u a0 = *(LAS v2u*)(S + L_PART + row * 1024 + L * 8), a1 = *(LAS v2u*)(S + L_PART + row * 1024 + 448 + L * 8);
      float v[8] = {blo(a0.x), bhi(a0.x), blo(a0.y), bhi(a0.y), blo(a1.x), bhi(a1.x), blo(a1.y), bhi(a1.y)};
      LAS char* arow = S + L_ACT + row * AS;
      if (isP0) {
        *(LAS v2u*)(arow + L * 8) = v2u{pk(silu(v[0]), silu(v[1])), pk(silu(v[2]), silu(v[3]))};
        *(LAS v2u*)(arow + 448 + L * 8) = v2u{pk(silu(v[4]), silu(v[5])), pk(silu(v[6]), silu(v[7]))};
      } else {
        ln_norm(v, act);
#pragma unroll
        for (int i = 0; i < 8; ++i) PIN(v[i]);
        v4u m[4];
        lwait(FL + F_TILE, 4);
        poll<4, 896, false>(m, ws, WS_MOD + (row * 672 + L) * 16, tag);
        mod_store(v, m, arow, L);
      }
      lset(FL + F_ACT, row);
      STAMP(1);
      if (isP1 || urgent) lwait(FL + F_GO, 4);
    }
  }

  if (w >= nh) {
    const int slot = w - nh, u = u0 + slot;
    const size_t kvo = ((size_t)(b * 8 + x) * 4096 + 3072 + u * 32) * 512;
    const rsrc_t rk = mk(kc + kvo, 16384), rv = mk(vc + kvo, 16384);
    const int rot = blk * 5 + (blk >> 4) + w * 3, rk4 = rot & 15, rv4 = (rot >> 1) & 15;
    v4u ka[16], va[16];
    {
      if (!isP) __builtin_amdgcn_s_sleep(DLY_KV);
      __builtin_amdgcn_s_setprio((ABL & 8) ? 0 : 1);
      const int kb = (n >> 1) * 512 + ((n & 1) + 2 * g) * 16, ks = (rk4 >> 2) * 4096 + (rk4 & 3) * 128;
#pragma unroll
      for (int i = 0; i < 16; ++i) {
        ka[i] = __builtin_amdgcn_raw_buffer_load_b128(rk, (ABL & 32) ? g * 4096 + n * 16 : kb, (ABL & 32) ? (rk4 ^ i) * 256 : ks ^ ((i >> 2) * 4096 + (i & 3) * 128), 2);
        __builtin_amdgcn_sched_barrier(0);
      }
      __builtin_amdgcn_s_setprio(0);
      const int vb = g * 4096 + n * 16, vs = (rv4 & 7) * 512 + (rv4 >> 3) * 256;
#pragma unroll
      for (int i = 0; i < 16; ++i) {
        va[i] = __builtin_amdgcn_raw_buffer_load_b128(rv, vb, vs ^ ((ABL & 16) ? i * 256 : ((i & 7) * 512 + (i >> 3) * 256)), 2);
        __builtin_amdgcn_sched_barrier(0);
      }
    }
    lset(FL + F_ISS, w);
    STAMP(2);
    lwait(FL + F_Q, 1);
    v4u qf[4];
#pragma unroll
    for (int i = 0; i < 4; ++i) qf[i] = *(LAS v4u*)(S + L_Q + ((n >> 1) & 1) * 512 + ((i ^ (rk4 & 3)) * 128) + ((n & 1) + 2 * g) * 16);
    f4 sa[4];
#pragma unroll
    for (int t = 0; t < 4; ++t) {
      sa[t] = f4{0.f, 0.f, 0.f, 0.f};
#pragma unroll
      for (int i = 0; i < 4; ++i) sa[t] = mfma(ka[4 * t + i], qf[i], sa[t]);
    }
    const int c = n >> 2, par = n & 1;
    const f4 sc = c == 0 ? sa[0] : c == 1 ? sa[1] : c == 2 ? sa[2] : sa[3];
    const float own = par ? sc.w : sc.x, snd = par ? sc.y : sc.z;
    const float s2 = rbf(own + dpp<XOR1>(snd)) * (0.0625f * 1.4426950408889634f);
    float mx = fmaxf(s2, dpp<XOR1>(s2));
    mx = fmaxf(mx, dpp<ROR4>(mx)); mx = fmaxf(mx, dpp<ROR8>(mx)); mx = rows_max(mx);
    const float p = __builtin_amdgcn_exp2f(s2 - mx);
    float l = p + dpp<XOR1>(p);
    l += dpp<ROR4>(l); l += dpp<ROR8>(l); l = rows_sum(l);
    LAS char* pb = S + L_P + slot * 128;
    *(LAS __bf16*)(pb + ((n >> 1) & 1) * 64 + ((c ^ (rk4 >> 2)) * 8 + ((2 * g + par) ^ (rv4 & 7))) * 2) = (__bf16)p;
    if (lane == 0 || lane == 2) *(LAS f2*)(S + L_STAT + slot * 16 + lane * 4) = f2{mx, l};
    lset(FL + F_SM, w);
    const v4u pa = *(volatile LAS v4u*)(pb + (n >> 3) * 64 + g * 16);
    unsigned po = L_PART + slot * 2048 + (g >> 1) * 1024 + (rv4 >> 3) * 512 + (g & 1) * 256 + n * 16;
    PIN(po);
    v4u am[8];
#pragma unroll
    for (int cc = 0; cc < 8; ++cc) {
      const bool on = (n & 7) == cc;
      am[cc] = v4u{on ? pa.x : 0u, on ? pa.y : 0u, on ? pa.z : 0u, on ? pa.w : 0u};
      PIN(am[cc]);
    }
    STAMP(3);
#pragma unroll
    for (int hs = 0; hs < 2; ++hs) {
      v4u f[8];
#pragma unroll
      for (int jj = 0; jj < 4; ++jj) {
#pragma unroll
        for (int cc = 0; cc < 8; ++cc) f[cc][jj] = __builtin_amdgcn_perm(va[8 * hs + 2 * jj + 1][cc >> 1], va[8 * hs + 2 * jj][cc >> 1], (cc & 1) ? 0x07060302u : 0x05040100u);
        if (jj < 3) __builtin_amdgcn_sched_barrier(0);
      }
      f4 o = {0.f, 0.f, 0.f, 0.f};
#pragma unroll
      for (int cc = 0; cc < 8; ++cc) o = mfma(am[cc], f[cc], o);
      *(LAS f4*)(S + (po ^ (hs * 512))) = o;
      __builtin_amdgcn_sched_barrier(0);
    }
    lset(FL + F_PV, w);
    __builtin_amdgcn_s_setprio(3);
    STAMP(4);
  } else {
    if (w == 0) {
      v4u q[2];
      poll<2, 1024, false>(q, ws, WS_Q + (b * 1024 + x * 128 + lane) * 16, tag);
#pragma unroll
      for (int h = 0; h < 2; ++h)
        *(LAS v2u*)(S + L_Q + h * 512 + lane * 8) = v2u{__builtin_amdgcn_perm(q[h].y, q[h].x, 0x07060302u), __builtin_amdgcn_perm(q[h].w, q[h].z, 0x07060302u)};
      lset(FL + F_Q, 0);
      STAMP(2);
    }
    if (w < 2) {
      const int h = w;
      lwait(FL + F_SM, nun);
      const bool live = n < nun;
      const f2 ml = *(volatile LAS f2*)(S + L_STAT + (live ? n : 0) * 16 + h * 8);
      float M = fmaxf(ml.x, dpp<XOR1>(ml.x));
      M = fmaxf(M, dpp<XOR2>(M)); M = fmaxf(M, dpp<ROR4>(M)); M = fmaxf(M, dpp<ROR8>(M));
      const float be = live ? __builtin_amdgcn_exp2f(ml.x - M) : 0.f;
      float Ls = be * ml.y;
      Ls += dpp<XOR1>(Ls); Ls += dpp<XOR2>(Ls); Ls += dpp<ROR4>(Ls); Ls += dpp<ROR8>(Ls);
      const float wt = be * __builtin_amdgcn_rcpf(Ls);
      const unsigned lse = asu(M + __builtin_amdgcn_logf(Ls));
      unsigned g1 = __builtin_amdgcn_perm(lse, tag, 0x07060100u), g3 = __builtin_amdgcn_perm(lse, tag, 0x05040302u);
      float wk[10];
#pragma unroll
      for (int i = 0; i < 10; ++i) wk[i] = asf(__builtin_amdgcn_readlane(asu(wt), i));
      int aoff = WS_ATT + ((((x * 8 + b) * 2 + h) * 4 + j) * 64 + lane) * 16;
      unsigned pa0 = L_PART + h * 1024 + lane * 16;
      PIN(g1); PIN(g3); PIN(aoff); PIN(pa0);
      f4 a[10];
#pragma nounroll
      for (int it = 0; it < SPIN_L; ++it) {
        const unsigned done = FL[F_PV + n];
#pragma unroll
        for (int i = 0; i < 10; ++i) a[i] = *(volatile LAS f4*)(S + pa0 + (i < 4 || i < nun ? i * 2048 : 0));
        if (lseen(done) >= nun) break;
      }
      const f4 acc = ((a[0] * wk[0] + a[1] * wk[1]) + (a[2] * wk[2] + a[3] * wk[3])) + (((a[4] * wk[4] + a[5] * wk[5]) + (a[6] * wk[6] + a[7] * wk[7])) + (a[8] * wk[8] + a[9] * wk[9]));
      put(ws, aoff, v4u{pk(acc.x, acc.y), g1, pk(acc.z, acc.w), g3});
      STAMP(4);
    }
  }

  if (isS1) {
    if (w >= 4) {
      const int bb = w - 4;
      unsigned so = L_ACT + bb * AS + (lane >> 5) * 256 + n * 16 + (g & 1) * 8;
      PIN(so);
      lwait(FL + F_PV, nun);
      v4u pq[8];
      poll<8, 1024, true>(pq, ws, WS_ATT + ((x * 8 + bb) * 512 + lane) * 16, tag);
#pragma unroll
      for (int h = 0; h < 2; ++h) {
        const v4u* pr = pq + 4 * h;
        const float l0 = lseA(pr[0]), l1 = lseA(pr[1]), l2 = lseA(pr[2]), l3 = lseA(pr[3]);
        const float M = fmaxf(fmaxf(l0, l1), fmaxf(l2, l3));
        const float e0 = __builtin_amdgcn_exp2f(l0 - M), e1 = __builtin_amdgcn_exp2f(l1 - M), e2 = __builtin_amdgcn_exp2f(l2 - M), e3 = __builtin_amdgcn_exp2f(l3 - M);
        const float inv = __builtin_amdgcn_rcpf((e0 + e1) + (e2 + e3));
        const float o0 = (e0 * blo(pr[0].x) + e1 * blo(pr[1].x)) + (e2 * blo(pr[2].x) + e3 * blo(pr[3].x));
        const float o1 = (e0 * bhi(pr[0].x) + e1 * bhi(pr[1].x)) + (e2 * bhi(pr[2].x) + e3 * bhi(pr[3].x));
        const float o2 = (e0 * blo(pr[0].z) + e1 * blo(pr[1].z)) + (e2 * blo(pr[2].z) + e3 * blo(pr[3].z));
        const float o3 = (e0 * bhi(pr[0].z) + e1 * bhi(pr[1].z)) + (e2 * bhi(pr[2].z) + e3 * bhi(pr[3].z));
        *(LAS v2u*)(S + so + h * 512) = v2u{pk(o0 * inv, o1 * inv), pk(o2 * inv, o3 * inv)};
      }
      STAMP(5);
      BARRIER();
    } else {
      v4u wf[16];
      unsigned tb = L_TILB + tr_base(lane, w);
      PIN(tb);
#pragma unroll
      for (int ks = 0; ks < 16; ++ks) wf[ks] = trfrag(S + tb + ks * 4096);
      unsigned ao = L_ACT + (n & 7) * AS + g * 16;
      int off = n < 8 ? WS_S1P + (((n * 7 + b) * 8 + x) * 16 + w * 4 + g) * 16 : 0x7ffffff0;
      PIN(ao); PIN(off);
      STAMP(5);
      BARRIER();
      v4u bq[16];
#pragma unroll
      for (int ks = 0; ks < 16; ++ks) bq[ks] = *(LAS v4u*)(S + ao + ks * 64);
      __builtin_amdgcn_sched_barrier(0);
      f4 a0 = {0.f, 0.f, 0.f, 0.f};
#pragma unroll
      for (int ks = 0; ks < 16; ++ks) a0 = mfma(wf[ks], bq[ks], a0);
      put(ws, off, enc4(a0, tag));
      STAMP(6);
    }
  } else if (isF) {
    if (w < 4) {
      v4u f1[14], f3[14];
      lwait(FL + F_TILE, 4);
      unsigned tb = L_TILE + tr_base(lane, w), tb3 = tb + L_TILB;
      PIN(tb); PIN(tb3);
#pragma unroll
      for (int ks = 0; ks < 14; ++ks) { f1[ks] = trfrag(S + tb + ks * 4096); f3[ks] = trfrag(S + tb3 + ks * 4096); }
      unsigned ao = L_ACT + (n & 7) * AS + g * 16, ho = L_HH + (n & 7) * HS + w * 32 + g * 8;
      PIN(ao); PIN(ho);
      STAMP(5);
      BARRIER();
      v4u bq[14];
#pragma unroll
      for (int ks = 0; ks < 7; ++ks) bq[ks] = *(LAS v4u*)(S + ao + ks * 64);
      __builtin_amdgcn_sched_barrier(0);
      f4 ga = {0.f, 0.f, 0.f, 0.f}, ua = ga;
#pragma unroll
      for (int ks = 0; ks < 14; ++ks) {
        ga = mfma(f1[ks], bq[ks], ga); ua = mfma(f3[ks], bq[ks], ua);
        if (ks < 7) bq[ks + 7] = *(LAS v4u*)(S + ao + (ks + 7) * 64);
        __builtin_amdgcn_sched_barrier(0);
      }
      float hh[4];
#pragma unroll
      for (int i = 0; i < 4; ++i) hh[i] = rbf(silu(rbf(ga[i]))) * rbf(ua[i]);
      *(LAS v2u*)(S + ho) = v2u{pk(hh[0], hh[1]), pk(hh[2], hh[3])};
      STAMP(6);
      BARRIER();
    } else {
      const int bb = w - 4;
      lwait(FL + F_ISS, nun);
      v2u wr[16];
      const rsrc_t r2 = mk(w2, (ABL & 4) ? 0 : 1792 * 896);
      const int wb = (fidx * 64 + 8 * g) * 896 + (bb < 7 ? bb : 6) * 128 + (4 * (n & 3) + (n >> 2)) * 8;
#pragma unroll
      for (int i = 0; i < 16; ++i) wr[i] = __builtin_amdgcn_raw_buffer_load_b64(r2, wb + ((i >> 3) * 32 + (i & 7)) * 896, 0, 2);
      v4u m[4];
      poll<4, 896, false>(m, ws, WS_MOD + (bb * 672 + 336 + L) * 16, tag);
      v4u f2_[8];
#pragma unroll
      for (int t = 0; t < 2; ++t)
#pragma unroll
        for (int cc = 0; cc < 4; ++cc)
#pragma unroll
          for (int jj = 0; jj < 4; ++jj) f2_[t * 4 + cc][jj] = __builtin_amdgcn_perm(wr[8 * t + 2 * jj + 1][cc >> 1], wr[8 * t + 2 * jj][cc >> 1], (cc & 1) ? 0x07060302u : 0x05040100u);
#pragma unroll
      for (int i = 0; i < 8; ++i) PIN(f2_[i]);
      f4 sh[2], sc[2];
#pragma unroll
      for (int i = 0; i < 8; ++i) { sh[i >> 2][i & 3] = bhi(m[i >> 2][i & 3]); sc[i >> 2][i & 3] = rbf(1.0f + bhi(m[2 + (i >> 2)][i & 3])); }
      unsigned ao = L_ACT + bb * AS + L * 8, ho = L_HH + (n & 7) * HS + g * 16;
      float cnt = act ? 1.0f / D : 0.f;
      int off = WS_S3P + ((((n & 7) * 7 + bb) * 28 + fidx) * 16 + (n >> 3) * 4 + g) * 16;
      PIN(sh[0]); PIN(sh[1]); PIN(sc[0]); PIN(sc[1]); PIN(ao); PIN(ho); PIN(off); PIN(cnt);
      lwait(FL + F_PV, nun);
      STAMP(5);
      v4u xg[2];
      poll<2, 896, false>(xg, ws, WS_X1 + (bb * 112 + L) * 16, tag);
      STAMP(6);
      f4 v0 = {bhi(xg[0].x), bhi(xg[0].y), bhi(xg[0].z), bhi(xg[0].w)}, v1 = {bhi(xg[1].x), bhi(xg[1].y), bhi(xg[1].z), bhi(xg[1].w)};
      f4 t = v0 + v1;
      const float mean = wave_sum(((t.x + t.y) + (t.z + t.w)) * cnt);
      const f4 d0 = v0 - mean, d1 = v1 - mean;
      t = d0 * d0 + d1 * d1;
      const float var = rbf(wave_sum(((t.x + t.y) + (t.z + t.w)) * cnt));
      const float mb = rbf(mean);
      v0 = (v0 - mb) * sc[0]; v1 = (v1 - mb) * sc[1];
      const float inv = __builtin_amdgcn_rcpf(rbf(__builtin_amdgcn_sqrtf(rbf(var + 1e-5f))));
      v0 = v0 * inv + sh[0]; v1 = v1 * inv + sh[1];
      *(LAS v2u*)(S + ao) = v2u{pk(v0.x, v0.y), pk(v0.z, v0.w)};
      *(LAS v2u*)(S + ao + 448) = v2u{pk(v1.x, v1.y), pk(v1.z, v1.w)};
      asm volatile("s_waitcnt lgkmcnt(0)\n\ts_barrier\n\ts_barrier" ::: "memory");
      if (bb < 7) {
        const v4u h0 = *(LAS v4u*)(S + ho), h1 = *(LAS v4u*)(S + ho + 64);
        f4 d[4];
#pragma unroll
        for (int cc = 0; cc < 4; ++cc) d[cc] = mfma(f2_[4 + cc], h1, mfma(f2_[cc], h0, f4{0.f, 0.f, 0.f, 0.f}));
        const bool hi = n >> 3;
        put(ws, off, enc4(f4{hi ? d[0].y : d[0].x, hi ? d[1].y : d[1].x, hi ? d[2].y : d[2].x, hi ? d[3].y : d[3].x}, tag));
        put(ws, off + 128, enc4(f4{hi ? d[0].w : d[0].z, hi ? d[1].w : d[1].z, hi ? d[2].w : d[2].z, hi ? d[3].w : d[3].z}, tag));
      }
      STAMP(7);
    }
  } else if (j == 2 && x < 7 && w == 2) {
      const v2u xv = __builtin_amdgcn_raw_buffer_load_b64(mk(xin, 8 * D * 2), b * 896 + x * 128 + n * 8, 0, 0);
      lwait(FL + F_PV, nun);
      v4u gg[2];
      poll<2, 336 * 16, false>(gg, ws, WS_MOD + (b * 672 + 224 + x * 16 + n) * 16, tag);
      f4 xf = {blo(xv.x), bhi(xv.x), blo(xv.y), bhi(xv.y)}, gaf = {bhi(gg[0].x), bhi(gg[0].y), bhi(gg[0].z), bhi(gg[0].w)}, gbf = {bhi(gg[1].x), bhi(gg[1].y), bhi(gg[1].z), bhi(gg[1].w)};
      int o1 = lane < 16 ? WS_X1 + (b * 112 + x * 16 + n) * 16 : 0x7ffffff0, o3 = WS_S3P + ((b * 7 + x) * 448 + lane) * 16, oo = lane < 16 ? b * 896 + x * 128 + n * 8 : 0x7ffffff0;
      PIN(xf); PIN(gaf); PIN(gbf); PIN(o1); PIN(o3); PIN(oo);
      v4u p1[2];
      poll<2, 1024, false>(p1, ws, WS_S1P + ((b * 7 + x) * 128 + lane) * 16, tag);
      STAMP(1);
      f4 a = dec4(p1[0]) + dec4(p1[1]);
      f4 x1;
#pragma unroll
      for (int i = 0; i < 4; ++i) x1[i] = rbf(xf[i] + rbf(gaf[i] * rbf(rows_sum(a[i]))));
      put(ws, o1, enc4b(x1, tag));
      STAMP(5);
      v4u p3[7];
      poll<7, 1024, false>(p3, ws, o3, tag);
      STAMP(6);
      a = ((dec4(p3[0]) + dec4(p3[1])) + (dec4(p3[2]) + dec4(p3[3]))) + ((dec4(p3[4]) + dec4(p3[5])) + dec4(p3[6]));
      f4 y;
#pragma unroll
      for (int i = 0; i < 4; ++i) y[i] = x1[i] + rbf(gbf[i] * rbf(rows_sum(a[i])));
      __builtin_amdgcn_raw_buffer_store_b64(v2u{pk(y[0], y[1]), pk(y[2], y[3])}, mk(out, 8 * D * 2), oo, 0, 0);
      if (idx == 0 && lane == 0) __builtin_amdgcn_raw_buffer_store_b32(tag, ws, WS_EPOCH, 0, 16);
      STAMP(7);
  }
}

#undef kc
#undef vc
#undef ws_
#undef wmod
#undef wq
#undef cin
#undef xin
#undef wo
#undef w1
#undef w3
#undef w2
#undef out
#undef tag

extern "C" void kernel_launch(void* const* d_in, const int* in_sizes, int n_in, void* d_out, int out_size, void* d_ws, size_t ws_size,
                              hipStream_t stream) {
  fused_decode<<<256, 768, 0, stream>>>((const char*)d_in[1], (const char*)d_in[2], (char*)d_ws, (const char*)d_in[10], (const char*)d_in[3],
                                        (const char*)d_in[9], (const char*)d_in[0], (const char*)d_in[4], (const char*)d_in[6],
                                        (const char*)d_in[7], (const char*)d_in[8], (char*)d_out);
}
```

```cpp
#define STAMPS 1
#define PROBE 0
#define PROBE_COND (true)
#include <hip/hip_runtime.h>
#include <stdint.h>

#define GAS __attribute__((address_space(1)))
#define LAS __attribute__((address_space(3)))
typedef unsigned v4u __attribute__((ext_vector_type(4)));
typedef unsigned v2u __attribute__((ext_vector_type(2)));
typedef float f4 __attribute__((ext_vector_type(4)));
typedef float f2 __attribute__((ext_vector_type(2)));
typedef __bf16 bf8 __attribute__((ext_vector_type(8)));
typedef __bf16 bf2 __attribute__((ext_vector_type(2)));
typedef short s4 __attribute__((ext_vector_type(4)));
typedef unsigned long long u64;
typedef __amdgpu_buffer_rsrc_t rsrc_t;

namespace {
constexpr int D = 448;
constexpr int WS_EPOCH = 0;
constexpr int WS_MOD = 4096;
constexpr int WS_Q = WS_MOD + 8 * 672 * 16;
constexpr int WS_ATT = WS_Q + 8 * 1024 * 16;
constexpr int WS_S1P = WS_ATT + 16 * 8 * 4 * 64 * 16;
constexpr int WS_X1 = WS_S1P + 8 * 7 * 8 * 16 * 16;
constexpr int WS_S3P = WS_X1 + 8 * 112 * 16;
constexpr int WS_END = WS_S3P + 8 * 7 * 28 * 16 * 16;
constexpr int WS_TS = 4 << 20;
constexpr int L_TILE = 0;
constexpr int L_TILB = 57344;
constexpr int L_PART = L_TILB + 65536;
constexpr int L_ACT = L_PART + 10 * 2048;
constexpr int AS = 1056;
constexpr int L_HH = L_ACT + 8 * AS;
constexpr int HS = 160;
constexpr int L_Q = L_HH + 8 * HS;
constexpr int L_P = L_Q + 1024;
constexpr int L_STAT = L_P + 10 * 128;
constexpr int L_FLAG = L_STAT + 10 * 16;
constexpr int L_DEAD = L_FLAG + 512;
constexpr int L_END = L_DEAD + 2048;
constexpr int F_SM = 0, F_PV = 16, F_Q = 32, F_ISS = 48, F_TILE = 64, F_ACT = 80, F_GO = 96;
#define DLY_P 5
#define DLY_KV 18
#define NUN 0xAA57
#define ABL 0
#define PFN 6
constexpr int CODE_KB = STAMPS ? 18 : 17;
constexpr int SPIN_G = 1 << 20, SPIN_L = 1 << 22;

__device__ __forceinline__ rsrc_t mk(const void* p, int n) { return __builtin_amdgcn_make_buffer_rsrc((void*)p, 0, n, 0x00020000); }
__device__ __forceinline__ float asf(unsigned u) { return __builtin_bit_cast(float, u); }
__device__ __forceinline__ unsigned asu(float f) { return __builtin_bit_cast(unsigned, f); }
__device__ __forceinline__ unsigned pk(float lo, float hi) { bf2 v = {(__bf16)lo, (__bf16)hi}; return __builtin_bit_cast(unsigned, v); }
__device__ __forceinline__ float rbf(float f) { return (float)(__bf16)f; }
__device__ __forceinline__ float blo(unsigned u) { return asf(u << 16); }
__device__ __forceinline__ float bhi(unsigned u) { return asf(u & 0xffff0000u); }
__device__ __forceinline__ f4 mfma(v4u a, v4u b, f4 c) {
  return __builtin_amdgcn_mfma_f32_16x16x32_bf16(__builtin_bit_cast(bf8, a), __builtin_bit_cast(bf8, b), c, 0, 0, 0);
}
template <int CTRL> __device__ __forceinline__ float dpp(float x) { return asf(__builtin_amdgcn_mov_dpp(asu(x), CTRL, 0xf, 0xf, true)); }
constexpr int XOR1 = 0xB1, XOR2 = 0x4E, ROR4 = 0x124, ROR8 = 0x128;
__device__ __forceinline__ float rows_sum(float x) {
  auto s = __builtin_amdgcn_permlane16_swap(asu(x), asu(x), false, false);
  x = asf(s[0]) + asf(s[1]);
  auto t = __builtin_amdgcn_permlane32_swap(asu(x), asu(x), false, false);
  return asf(t[0]) + asf(t[1]);
}
__device__ __forceinline__ float rows_max(float x) {
  auto s = __builtin_amdgcn_permlane16_swap(asu(x), asu(x), false, false);
  x = fmaxf(asf(s[0]), asf(s[1]));
  auto t = __builtin_amdgcn_permlane32_swap(asu(x), asu(x), false, false);
  return fmaxf(asf(t[0]), asf(t[1]));
}
__device__ __forceinline__ float wave_sum(float x) {
  x += dpp<XOR1>(x); x += dpp<XOR2>(x); x += dpp<ROR4>(x); x += dpp<ROR8>(x);
  return rows_sum(x);
}
#define CBAR() asm volatile("" ::: "memory")
__device__ __forceinline__ u64 rt() { u64 t; __builtin_amdgcn_sched_barrier(0); asm volatile("s_memrealtime %0\n\ts_waitcnt lgkmcnt(0)" : "=s"(t) :: "memory"); __builtin_amdgcn_sched_barrier(0); return t; }
#define STAMP(i) st[i] = rt()

__device__ __forceinline__ v4u enc4(f4 v, unsigned tag) {
  v4u r;
  r.x = __builtin_amdgcn_perm(tag, asu(v.x) + 0x80u, 0x03020104u);
  r.y = __builtin_amdgcn_perm(tag, asu(v.y) + 0x80u, 0x03020105u);
  r.z = __builtin_amdgcn_perm(tag, asu(v.z) + 0x80u, 0x03020106u);
  r.w = __builtin_amdgcn_perm(tag, asu(v.w) + 0x80u, 0x03020107u);
  return r;
}
__device__ __forceinline__ v4u enc4b(f4 v, unsigned tag) {
  return v4u{__builtin_amdgcn_perm(tag, asu(v.x), 0x03020104u), __builtin_amdgcn_perm(tag, asu(v.y), 0x03020105u),
             __builtin_amdgcn_perm(tag, asu(v.z), 0x03020106u), __builtin_amdgcn_perm(tag, asu(v.w), 0x03020107u)};
}
__device__ __forceinline__ unsigned tag4(v4u g) {
  return __builtin_amdgcn_perm(g.y, g.x, 0x0c0c0400u) | __builtin_amdgcn_perm(g.w, g.z, 0x04000c0cu);
}
__device__ __forceinline__ f4 dec4(v4u g) { return f4{asf(g.x & ~255u), asf(g.y & ~255u), asf(g.z & ~255u), asf(g.w & ~255u)}; }
__device__ __forceinline__ void put(rsrc_t ws, int off, v4u g) { __builtin_amdgcn_raw_buffer_store_b128(g, ws, off, 0, 16); }
__device__ __forceinline__ unsigned tagA(v4u g) { return __builtin_amdgcn_perm(g.w, g.y, 0x05040100u); }
__device__ __forceinline__ float lseA(v4u g) { return asf(__builtin_amdgcn_perm(g.y, g.w, 0x07060302u)); }

template <int N, int STEP, bool ATT>
__device__ __forceinline__ void poll(v4u (&x)[N], rsrc_t ws, int off, unsigned tag) {
#pragma nounroll
  for (int it = 0; it < SPIN_G; ++it) {
    CBAR();
#pragma unroll
    for (int l = 0; l < N; ++l) x[l] = __builtin_amdgcn_raw_buffer_load_b128(ws, off + l * STEP, 0, 16);
    unsigned bad = 0;
#pragma unroll
    for (int l = 0; l < N; ++l) bad |= (ATT ? tagA(x[l]) : tag4(x[l])) ^ tag;
    if (__builtin_amdgcn_ballot_w64(bad != 0) == 0) break;
  }
}
__device__ __forceinline__ int lseen(unsigned v) { return __builtin_popcount((unsigned)__builtin_amdgcn_ballot_w64(v != 0u) & 0xffffu); }
__device__ __forceinline__ void lwait(volatile LAS unsigned* f, int want) {
  volatile LAS unsigned* p = f + (threadIdx.x & 15);
#pragma nounroll
  for (int it = 0; it < SPIN_L; ++it) {
    if (lseen(*p) >= want) break;
    __builtin_amdgcn_s_sleep(1);
  }
  CBAR();
}
__device__ __forceinline__ void lset(volatile LAS unsigned* f, int w) { CBAR(); f[w] = 1u; }
template <bool NT>
__device__ __forceinline__ void dma(rsrc_t rs, int voff, int soff, unsigned ldsaddr) {
  unsigned keep;
  if (NT)
    asm volatile("s_mov_b32 %0, m0\n\ts_mov_b32 m0, %3\n\ts_nop 0\n\tbuffer_load_dwordx4 %1, %2, %4 offen nt lds\n\ts_mov_b32 m0, %0"
                 : "=&s"(keep) : "v"(voff), "s"(rs), "s"(ldsaddr), "s"(soff) : "memory");
  else
    asm volatile("s_mov_b32 %0, m0\n\ts_mov_b32 m0, %3\n\ts_nop 0\n\tbuffer_load_dwordx4 %1, %2, %4 offen lds\n\ts_mov_b32 m0, %0"
                 : "=&s"(keep) : "v"(voff), "s"(rs), "s"(ldsaddr), "s"(soff) : "memory");
}
template <bool NT>
__device__ __forceinline__ void dma_tile(rsrc_t rs, int base, int stride, int q0, int dq, int n, unsigned lds, int dl, int lane) {
  const int rowoff = (lane >> 3) * stride;
  const int v0 = rowoff + (((lane & 7) ^ (((lane >> 4) & 1) << 1)) << 4), v1 = v0 ^ 64;
  int so = base + q0 * 8 * stride;
  unsigned la = lds;
#pragma nounroll
  for (int i = 0; i < n; ++i) {
    dma<NT>(rs, v0, so, la);
    dma<NT>(rs, v1, so + 8 * stride, la + 1024);
    so += dq * 8 * stride; la += dl;
  }
}
__device__ __forceinline__ v4u trfrag(LAS char* p) {
  s4 a = __builtin_amdgcn_ds_read_tr16_b64_v4i16((LAS s4*)p), b = __builtin_amdgcn_ds_read_tr16_b64_v4i16((LAS s4*)(p + 512));
  v2u x = __builtin_bit_cast(v2u, a), y = __builtin_bit_cast(v2u, b);
  return v4u{x.x, x.y, y.x, y.y};
}
__device__ __forceinline__ int tr_base(int lane, int nt) {
  const int g = lane >> 4, q4 = (lane & 15) >> 2, p = lane & 3;
  return (8 * g + q4) * 128 + ((nt ^ ((q4 >> 1) | ((g & 1) << 1))) << 5) + p * 8;
}
__device__ __forceinline__ void ln_norm(float (&x)[8], bool act) {
  float s = 0.f;
#pragma unroll
  for (int i = 0; i < 8; ++i) s += x[i];
  const float mean = wave_sum(act ? s : 0.f) * (1.0f / D);
  float q = 0.f;
#pragma unroll
  for (int i = 0; i < 8; ++i) { const float d = x[i] - mean; q += d * d; }
  const float var = rbf(wave_sum(act ? q : 0.f) * (1.0f / D));
  const float mb = rbf(mean);
  const float inv = __builtin_amdgcn_rcpf(rbf(__builtin_amdgcn_sqrtf(rbf(var + 1e-5f))));
#pragma unroll
  for (int i = 0; i < 8; ++i) x[i] = rbf(rbf(x[i] - mb) * inv);
}
__device__ __forceinline__ void mod_store(const float (&ln)[8], const v4u (&m)[4], LAS char* row, int L) {
  float h[8];
#pragma unroll
  for (int i = 0; i < 8; ++i) {
    const float sh = bhi(m[i >> 2][i & 3]), sc = bhi(m[2 + (i >> 2)][i & 3]);
    h[i] = rbf(ln[i] * rbf(1.0f + sc)) + sh;
  }
  *(LAS v2u*)(row + L * 8) = v2u{pk(h[0], h[1]), pk(h[2], h[3])};
  *(LAS v2u*)(row + 448 + L * 8) = v2u{pk(h[4], h[5]), pk(h[6], h[7])};
}
__device__ __forceinline__ float silu(float v) {
  const float d = 1.0f + __builtin_amdgcn_exp2f(fminf(-1.4426950408889634f * v, 126.0f)), r = __builtin_amdgcn_rcpf(d), q = v * r;
  return __builtin_fmaf(__builtin_fmaf(-q, d, v), r, q);
}
#define PIN(x) asm volatile("" : "+v"(x))
#define BARRIER() asm volatile("s_waitcnt lgkmcnt(0)\n\ts_barrier" ::: "memory")
}

typedef unsigned s16u __attribute__((ext_vector_type(16)));
typedef unsigned s8u __attribute__((ext_vector_type(8)));
#define KPTR(A, i) ((char*)(((u64)(A)[2 * (i) + 1] << 32) | (A)[2 * (i)]))
#define kc KPTR(KA, 0)
#define vc KPTR(KA, 1)
#define ws_ KPTR(KA, 2)
#define wmod KPTR(KA, 3)
#define wq KPTR(KA, 4)
#define cin KPTR(KA, 5)
#define xin KPTR(KA, 6)
#define wo KPTR(KA, 7)
#define w1 KPTR(KB, 0)
#define w3 KPTR(KB, 1)
#define w2 KPTR(KB, 2)
#define out KPTR(KB, 3)
#define KWAIT() do { asm volatile("s_waitcnt lgkmcnt(0) ; KWAIT" : "={s[72:87]}"(KA), "={s[88:95]}"(KB)); ws = mk(ws_, WS_END); \
    int zoff = WS_EPOCH; PIN(zoff); epoch = __builtin_amdgcn_raw_buffer_load_b32(ws, zoff, 0, 16); } while (0)
#define tag (epoch + 0x01010101u)

__global__ void __launch_bounds__(768, 3)
fused_decode(const char*, const char*, char*, const char*, const char*, const char*, const char*, const char*, const char*, const char*, const char*, char*) {
  __shared__ __attribute__((aligned(16))) char smem[L_END];
  LAS char* S = (LAS char*)smem;
  volatile LAS unsigned* FL = (volatile LAS unsigned*)(S + L_FLAG);
  u64 st[8] = {0, 0, 0, 0, 0, 0, 0, 0};
  STAMP(0);
  const int blk = blockIdx.x, lane = threadIdx.x & 63, w = __builtin_amdgcn_readfirstlane(threadIdx.x >> 6);
  const unsigned lb = (unsigned)(size_t)S;
  const int x = blk & 7, r = blk >> 3, b = r >> 2, j = r & 3;
  s16u KA; s8u KB;
  {
    u64 pc; unsigned keep;
    const v2u zero = {0u, 0u};
    asm volatile(
        "s_cmp_lg_u32 %[w], 0\n\ts_cbranch_scc1 1f\n\t"
        "ds_write_b64 %[fa], %[z]\n\t"
        "s_getpc_b64 %[pc]\n\ts_mov_b32 %[k], m0\n\ts_mov_b32 m0, %[dead]\n\ts_nop 0\n\t"
        "global_load_lds_dwordx4 %[co], %[pc]\n\ts_mov_b32 m0, %[k]\n\t"
        "s_waitcnt lgkmcnt(0)\n"
        "1:\n\t"
        "s_load_dwordx16 s[72:87], %[ka], 0\n\ts_load_dwordx8 s[88:95], %[ka], 64\n\ts_barrier"
        : [pc] "=&s"(pc), [k] "=&s"(keep)
        : [w] "s"(w), [fa] "v"(lb + L_FLAG + lane * 8), [z] "v"(zero), [dead] "s"(lb + L_DEAD), [co] "v"((r < CODE_KB ? CODE_KB - 1 - r : 31 - r) * 1024 + 384 + lane * 16),
          [ka] "s"(__builtin_amdgcn_kernarg_segment_ptr())
        : "scc", "memory", "s72", "s73", "s74", "s75", "s76", "s77", "s78", "s79", "s80", "s81", "s82", "s83", "s84", "s85", "s86", "s87",
          "s88", "s89", "s90", "s91", "s92", "s93", "s94", "s95");
  }
  constexpr int N0 = NUN & 15, N1 = (NUN >> 4) & 15, N2 = (NUN >> 8) & 15;
  static_assert(N0 + N1 + N2 + (NUN >> 12) == 32 && N0 <= 8 && N1 <= 7 && N1 >= 4, "unit split");
  const int nun = (NUN >> (4 * j)) & 15, u0 = ((N0 << 8 | (N0 + N1) << 16 | (N0 + N1 + N2) << 24) >> (8 * j)) & 255, nh = 12 - nun;
  const int idx = b * 8 + x;
  const bool urgent = x < 2 && b < 7;
  const int k0 = x < 2 ? 48 + x : (x - 2) * 8 + b;
  const bool isP0 = j == 0 && (urgent || k0 < 14), isP1 = j == 1, isS1 = isP1 && b < 7;
  const bool isF = j == 0 && !urgent && k0 >= 14 && k0 < 42;
  const int fidx = k0 - 14;
  const int g = lane >> 4, n = lane & 15;
  rsrc_t ws;
  unsigned epoch;
  __builtin_amdgcn_s_setprio(3);
  const bool act = lane < 56;
  const int L = act ? lane : 55;
  const bool isP = isP0 || isP1, pf = j >= 2 && urgent && w < 2;
  const int tile = isP1 ? idx : isF ? fidx : urgent ? 2 * b + x : 14 + 2 * k0;
  KWAIT();
  if (isP && w >= 4) dma<false>(mk(isP0 ? cin : xin, 8 * D * 2), lane * 16, (w - 4) * 896, lb + L_PART + (w - 4) * 1024);
  if (!(urgent && j != 1)) __builtin_amdgcn_s_sleep(DLY_P);
  if (((isP || isF) && w < 4) || pf) {
    const int sa = isP1 ? 8192 : isF ? 3584 : 5376;
    dma_tile<true>(mk(isP1 ? wq : isF ? w1 : wmod, D * sa), tile * 128, sa, pf ? 56 - PFN * (2 * j + w - 3) : w * 2, pf ? 2 : 8, pf ? PFN / 2 : 7,
                   lb + (pf ? L_DEAD : L_TILE + w * 2048), pf ? 0 : 8192, lane);
    if (isS1 || (j == 0 && !urgent))
      dma_tile<true>(mk(isP1 ? wo : isF ? w3 : wmod, isP1 ? 4096 * 896 : D * sa), isP1 ? x * 512 * 896 + b * 128 : isF ? tile * 128 : tile * 128 + 128, isP1 ? 896 : sa,
                     w * 2, 8, isP1 ? 8 : 7, lb + L_TILB + w * 2048, 8192, lane);
    if (isF) {
      asm volatile("s_waitcnt vmcnt(0)" ::: "memory");
      lset(FL + F_TILE, w);
      STAMP(1);
    }
  }
  if (__builtin_expect(isP, 0)) {
    if (w < 4) {
      unsigned tb = L_TILE + tr_base(lane, w), ao = L_ACT + (n & 7) * AS + g * 16;
      int off = n >= 8 ? 0x7ffff000 : (isP0 ? WS_MOD + n * 672 * 16 : WS_Q + n * 1024 * 16) + (tile * 16 + w * 4 + g) * 16;
      PIN(tb); PIN(ao); PIN(off);
      v4u wf[14], bq[14];
      if (isP0) {
        lwait(FL + F_ACT, 8);
#pragma unroll
        for (int ks = 0; ks < 14; ++ks) bq[ks] = *(LAS v4u*)(S + ao + ks * 64);
      }
      asm volatile("s_waitcnt vmcnt(0)" ::: "memory");
      lset(FL + F_TILE, w);
      STAMP(1);
      lwait(FL + F_TILE, 4);
#pragma nounroll
      for (int t = (isP1 || urgent) ? 1 : 0; t < 2; ++t) {
#pragma unroll
        for (int ks = 0; ks < 14; ++ks) wf[ks] = trfrag(S + tb + ks * 4096);
        if (!isP0) {
          lwait(FL + F_ACT, 8);
#pragma unroll
          for (int ks = 0; ks < 14; ++ks) bq[ks] = *(LAS v4u*)(S + ao + ks * 64);
        }
        __builtin_amdgcn_sched_barrier(0);
        f4 acc = {0.f, 0.f, 0.f, 0.f};
#pragma unroll
        for (int ks = 0; ks < 14; ++ks) acc = mfma(wf[ks], bq[ks], acc);
        put(ws, off, enc4b(f4{rbf(acc.x), rbf(acc.y), rbf(acc.z), rbf(acc.w)}, tag));
        tb += L_TILB; off += 256;
      }
      lset(FL + F_GO, w);
      STAMP(7);
    } else {
      const int row = w - 4;
      asm volatile("s_waitcnt vmcnt(0)" ::: "memory");
      const v2u a0 = *(LAS v2u*)(S + L_PART + row * 1024 + L * 8), a1 = *(LAS v2u*)(S + L_PART + row * 1024 + 448 + L * 8);
      float v[8] = {blo(a0.x), bhi(a0.x), blo(a0.y), bhi(a0.y), blo(a1.x), bhi(a1.x), blo(a1.y), bhi(a1.y)};
      LAS char* arow = S + L_ACT + row * AS;
      if (isP0) {
        *(LAS v2u*)(arow + L * 8) = v2u{pk(silu(v[0]), silu(v[1])), pk(silu(v[2]), silu(v[3]))};
        *(LAS v2u*)(arow + 448 + L * 8) = v2u{pk(silu(v[4]), silu(v[5])), pk(silu(v[6]), silu(v[7]))};
      } else {
        ln_norm(v, act);
#pragma unroll
        for (int i = 0; i < 8; ++i) PIN(v[i]);
        v4u m[4];
        lwait(FL + F_TILE, 4);
        poll<4, 896, false>(m, ws, WS_MOD + (row * 672 + L) * 16, tag);
        mod_store(v, m, arow, L);
      }
      lset(FL + F_ACT, row);
      STAMP(1);
      if (isP1 || urgent) lwait(FL + F_GO, 4);
    }
  }

  if (w >= nh) {
    const int slot = w - nh, u = u0 + slot;
    const size_t kvo = ((size_t)(b * 8 + x) * 4096 + 3072 + u * 32) * 512;
    const rsrc_t rk = mk(kc + kvo, 16384), rv = mk(vc + kvo, 16384);
    const int rot = blk * 5 + (blk >> 4) + w * 3, rk4 = rot & 15, rv4 = (rot >> 1) & 15;
    v4u ka[16], va[16];
    {
      if (!isP) __builtin_amdgcn_s_sleep(DLY_KV);
      __builtin_amdgcn_s_setprio((ABL & 8) ? 0 : 1);
      const int kb = (n >> 1) * 512 + ((n & 1) + 2 * g) * 16, ks = (rk4 >> 2) * 4096 + (rk4 & 3) * 128;
#pragma unroll
      for (int i = 0; i < 16; ++i) {
        ka[i] = __builtin_amdgcn_raw_buffer_load_b128(rk, (ABL & 32) ? g * 4096 + n * 16 : kb, (ABL & 32) ? (rk4 ^ i) * 256 : ks ^ ((i >> 2) * 4096 + (i & 3) * 128), 2);
        __builtin_amdgcn_sched_barrier(0);
      }
      __builtin_amdgcn_s_setprio(0);
      const int vb = g * 4096 + n * 16, vs = (rv4 & 7) * 512 + (rv4 >> 3) * 256;
#pragma unroll
      for (int i = 0; i < 16; ++i) {
        va[i] = __builtin_amdgcn_raw_buffer_load_b128(rv, vb, vs ^ ((ABL & 16) ? i * 256 : ((i & 7) * 512 + (i >> 3) * 256)), 2);
        __builtin_amdgcn_sched_barrier(0);
      }
    }
    lset(FL + F_ISS, w);
    STAMP(2);
    lwait(FL + F_Q, 1);
    v4u qf[4];
#pragma unroll
    for (int i = 0; i < 4; ++i) qf[i] = *(LAS v4u*)(S + L_Q + ((n >> 1) & 1) * 512 + ((i ^ (rk4 & 3)) * 128) + ((n & 1) + 2 * g) * 16);
    f4 sa[4];
#pragma unroll
    for (int t = 0; t < 4; ++t) {
      sa[t] = f4{0.f, 0.f, 0.f, 0.f};
#pragma unroll
      for (int i = 0; i < 4; ++i) sa[t] = mfma(ka[4 * t + i], qf[i], sa[t]);
    }
    const int c = n >> 2, par = n & 1;
    const f4 sc = c == 0 ? sa[0] : c == 1 ? sa[1] : c == 2 ? sa[2] : sa[3];
    const float own = par ? sc.w : sc.x, snd = par ? sc.y : sc.z;
    const float s2 = rbf(own + dpp<XOR1>(snd)) * (0.0625f * 1.4426950408889634f);
    float mx = fmaxf(s2, dpp<XOR1>(s2));
    mx = fmaxf(mx, dpp<ROR4>(mx)); mx = fmaxf(mx, dpp<ROR8>(mx)); mx = rows_max(mx);
    const float p = __builtin_amdgcn_exp2f(s2 - mx);
    float l = p + dpp<XOR1>(p);
    l += dpp<ROR4>(l); l += dpp<ROR8>(l); l = rows_sum(l);
    LAS char* pb = S + L_P + slot * 128;
    *(LAS __bf16*)(pb + ((n >> 1) & 1) * 64 + ((c ^ (rk4 >> 2)) * 8 + ((2 * g + par) ^ (rv4 & 7))) * 2) = (__bf16)p;
    if (lane == 0 || lane == 2) *(LAS f2*)(S + L_STAT + slot * 16 + lane * 4) = f2{mx, l};
    lset(FL + F_SM, w);
    const v4u pa = *(volatile LAS v4u*)(pb + (n >> 3) * 64 + g * 16);
    unsigned po = L_PART + slot * 2048 + (g >> 1) * 1024 + (rv4 >> 3) * 512 + (g & 1) * 256 + n * 16;
    PIN(po);
    v4u am[8];
#pragma unroll
    for (int cc = 0; cc < 8; ++cc) {
      const bool on = (n & 7) == cc;
      am[cc] = v4u{on ? pa.x : 0u, on ? pa.y : 0u, on ? pa.z : 0u, on ? pa.w : 0u};
      PIN(am[cc]);
    }
    STAMP(3);
#pragma unroll
    for (int hs = 0; hs < 2; ++hs) {
      v4u f[8];
#pragma unroll
      for (int jj = 0; jj < 4; ++jj) {
#pragma unroll
        for (int cc = 0; cc < 8; ++cc) f[cc][jj] = __builtin_amdgcn_perm(va[8 * hs + 2 * jj + 1][cc >> 1], va[8 * hs + 2 * jj][cc >> 1], (cc & 1) ? 0x07060302u : 0x05040100u);
        if (jj < 3) __builtin_amdgcn_sched_barrier(0);
      }
      f4 o = {0.f, 0.f, 0.f, 0.f};
#pragma unroll
      for (int cc = 0; cc < 8; ++cc) o = mfma(am[cc], f[cc], o);
      *(LAS f4*)(S + (po ^ (hs * 512))) = o;
      __builtin_amdgcn_sched_barrier(0);
    }
    lset(FL + F_PV, w);
    __builtin_amdgcn_s_setprio(3);
    STAMP(4);
  } else {
    if (w == 0) {
      v4u q[2];
      poll<2, 1024, false>(q, ws, WS_Q + (b * 1024 + x * 128 + lane) * 16, tag);
#pragma unroll
      for (int h = 0; h < 2; ++h)
        *(LAS v2u*)(S + L_Q + h * 512 + lane * 8) = v2u{__builtin_amdgcn_perm(q[h].y, q[h].x, 0x07060302u), __builtin_amdgcn_perm(q[h].w, q[h].z, 0x07060302u)};
      lset(FL + F_Q, 0);
      STAMP(2);
    }
    if (w < 2) {
      const int h = w;
      lwait(FL + F_SM, nun);
      const bool live = n < nun;
      const f2 ml = *(volatile LAS f2*)(S + L_STAT + (live ? n : 0) * 16 + h * 8);
      float M = fmaxf(ml.x, dpp<XOR1>(ml.x));
      M = fmaxf(M, dpp<XOR2>(M)); M = fmaxf(M, dpp<ROR4>(M)); M = fmaxf(M, dpp<ROR8>(M));
      const float be = live ? __builtin_amdgcn_exp2f(ml.x - M) : 0.f;
      float Ls = be * ml.y;
      Ls += dpp<XOR1>(Ls); Ls += dpp<XOR2>(Ls); Ls += dpp<ROR4>(Ls); Ls += dpp<ROR8>(Ls);
      const float wt = be * __builtin_amdgcn_rcpf(Ls);
      const unsigned lse = asu(M + __builtin_amdgcn_logf(Ls));
      unsigned g1 = __builtin_amdgcn_perm(lse, tag, 0x07060100u), g3 = __builtin_amdgcn_perm(lse, tag, 0x05040302u);
      float wk[10];
#pragma unroll
      for (int i = 0; i < 10; ++i) wk[i] = asf(__builtin_amdgcn_readlane(asu(wt), i));
      int aoff = WS_ATT + ((((x * 8 + b) * 2 + h) * 4 + j) * 64 + lane) * 16;
      unsigned pa0 = L_PART + h * 1024 + lane * 16;
      PIN(g1); PIN(g3); PIN(aoff); PIN(pa0);
      f4 a[10];
#pragma nounroll
      for (int it = 0; it < SPIN_L; ++it) {
        const unsigned done = FL[F_PV + n];
#pragma unroll
        for (int i = 0; i < 10; ++i) a[i] = *(volatile LAS f4*)(S + pa0 + (i < 4 || i < nun ? i * 2048 : 0));
        if (lseen(done) >= nun) break;
      }
      const f4 acc = ((a[0] * wk[0] + a[1] * wk[1]) + (a[2] * wk[2] + a[3] * wk[3])) + (((a[4] * wk[4] + a[5] * wk[5]) + (a[6] * wk[6] + a[7] * wk[7])) + (a[8] * wk[8] + a[9] * wk[9]));
      put(ws, aoff, v4u{pk(acc.x, acc.y), g1, pk(acc.z, acc.w), g3});
      STAMP(4);
    }
  }

  if (isS1) {
    if (w >= 4) {
      const int bb = w - 4;
      unsigned so = L_ACT + bb * AS + (lane >> 5) * 256 + n * 16 + (g & 1) * 8;
      PIN(so);
      lwait(FL + F_PV, nun);
      v4u pq[8];
      poll<8, 1024, true>(pq, ws, WS_ATT + ((x * 8 + bb) * 512 + lane) * 16, tag);
#pragma unroll
      for (int h = 0; h < 2; ++h) {
        const v4u* pr = pq + 4 * h;
        const float l0 = lseA(pr[0]), l1 = lseA(pr[1]), l2 = lseA(pr[2]), l3 = lseA(pr[3]);
        const float M = fmaxf(fmaxf(l0, l1), fmaxf(l2, l3));
        const float e0 = __builtin_amdgcn_exp2f(l0 - M), e1 = __builtin_amdgcn_exp2f(l1 - M), e2 = __builtin_amdgcn_exp2f(l2 - M), e3 = __builtin_amdgcn_exp2f(l3 - M);
        const float inv = __builtin_amdgcn_rcpf((e0 + e1) + (e2 + e3));
        const float o0 = (e0 * blo(pr[0].x) + e1 * blo(pr[1].x)) + (e2 * blo(pr[2].x) + e3 * blo(pr[3].x));
        const float o1 = (e0 * bhi(pr[0].x) + e1 * bhi(pr[1].x)) + (e2 * bhi(pr[2].x) + e3 * bhi(pr[3].x));
        const float o2 = (e0 * blo(pr[0].z) + e1 * blo(pr[1].z)) + (e2 * blo(pr[2].z) + e3 * blo(pr[3].z));
        const float o3 = (e0 * bhi(pr[0].z) + e1 * bhi(pr[1].z)) + (e2 * bhi(pr[2].z) + e3 * bhi(pr[3].z));
        *(LAS v2u*)(S + so + h * 512) = v2u{pk(o0 * inv, o1 * inv), pk(o2 * inv, o3 * inv)};
      }
      STAMP(5);
      BARRIER();
    } else {
      v4u wf[16];
      unsigned tb = L_TILB + tr_base(lane, w);
      PIN(tb);
#pragma unroll
      for (int ks = 0; ks < 16; ++ks) wf[ks] = trfrag(S + tb + ks * 4096);
      unsigned ao = L_ACT + (n & 7) * AS + g * 16;
      int off = n < 8 ? WS_S1P + (((n * 7 + b) * 8 + x) * 16 + w * 4 + g) * 16 : 0x7ffffff0;
      PIN(ao); PIN(off);
      STAMP(5);
      BARRIER();
      v4u bq[16];
#pragma unroll
      for (int ks = 0; ks < 16; ++ks) bq[ks] = *(LAS v4u*)(S + ao + ks * 64);
      __builtin_amdgcn_sched_barrier(0);
      f4 a0 = {0.f, 0.f, 0.f, 0.f};
#pragma unroll
      for (int ks = 0; ks < 16; ++ks) a0 = mfma(wf[ks], bq[ks], a0);
      put(ws, off, enc4(a0, tag));
      STAMP(6);
    }
  } else if (isF) {
    if (w < 4) {
      v4u f1[14], f3[14];
      lwait(FL + F_TILE, 4);
      unsigned tb = L_TILE + tr_base(lane, w), tb3 = tb + L_TILB;
      PIN(tb); PIN(tb3);
#pragma unroll
      for (int ks = 0; ks < 14; ++ks) { f1[ks] = trfrag(S + tb + ks * 4096); f3[ks] = trfrag(S + tb3 + ks * 4096); }
      unsigned ao = L_ACT + (n & 7) * AS + g * 16, ho = L_HH + (n & 7) * HS + w * 32 + g * 8;
      PIN(ao); PIN(ho);
      STAMP(5);
      BARRIER();
      v4u bq[14];
#pragma unroll
      for (int ks = 0; ks < 7; ++ks) bq[ks] = *(LAS v4u*)(S + ao + ks * 64);
      __builtin_amdgcn_sched_barrier(0);
      f4 ga = {0.f, 0.f, 0.f, 0.f}, ua = ga;
#pragma unroll
      for (int ks = 0; ks < 14; ++ks) {
        ga = mfma(f1[ks], bq[ks], ga); ua = mfma(f3[ks], bq[ks], ua);
        if (ks < 7) bq[ks + 7] = *(LAS v4u*)(S + ao + (ks + 7) * 64);
        __builtin_amdgcn_sched_barrier(0);
      }
      float hh[4];
#pragma unroll
      for (int i = 0; i < 4; ++i) hh[i] = rbf(silu(rbf(ga[i]))) * rbf(ua[i]);
      *(LAS v2u*)(S + ho) = v2u{pk(hh[0], hh[1]), pk(hh[2], hh[3])};
      STAMP(6);
      BARRIER();
    } else {
      const int bb = w - 4;
      lwait(FL + F_ISS, nun);
      v2u wr[16];
      const rsrc_t r2 = mk(w2, (ABL & 4) ? 0 : 1792 * 896);
      const int wb = (fidx * 64 + 8 * g) * 896 + (bb < 7 ? bb : 6) * 128 + (4 * (n & 3) + (n >> 2)) * 8;
#pragma unroll
      for (int i = 0; i < 16; ++i) wr[i] = __builtin_amdgcn_raw_buffer_load_b64(r2, wb + ((i >> 3) * 32 + (i & 7)) * 896, 0, 2);
      v4u m[4];
      poll<4, 896, false>(m, ws, WS_MOD + (bb * 672 + 336 + L) * 16, tag);
      v4u f2_[8];
#pragma unroll
      for (int t = 0; t < 2; ++t)
#pragma unroll
        for (int cc = 0; cc < 4; ++cc)
#pragma unroll
          for (int jj = 0; jj < 4; ++jj) f2_[t * 4 + cc][jj] = __builtin_amdgcn_perm(wr[8 * t + 2 * jj + 1][cc >> 1], wr[8 * t + 2 * jj][cc >> 1], (cc & 1) ? 0x07060302u : 0x05040100u);
#pragma unroll
      for (int i = 0; i < 8; ++i) PIN(f2_[i]);
      f4 sh[2], sc[2];
#pragma unroll
      for (int i = 0; i < 8; ++i) { sh[i >> 2][i & 3] = bhi(m[i >> 2][i & 3]); sc[i >> 2][i & 3] = rbf(1.0f + bhi(m[2 + (i >> 2)][i & 3])); }
      unsigned ao = L_ACT + bb * AS + L * 8, ho = L_HH + (n & 7) * HS + g * 16;
      float cnt = act ? 1.0f / D : 0.f;
      int off = WS_S3P + ((((n & 7) * 7 + bb) * 28 + fidx) * 16 + (n >> 3) * 4 + g) * 16;
      PIN(sh[0]); PIN(sh[1]); PIN(sc[0]); PIN(sc[1]); PIN(ao); PIN(ho); PIN(off); PIN(cnt);
      lwait(FL + F_PV, nun);
      STAMP(5);
      v4u xg[2];
      poll<2, 896, false>(xg, ws, WS_X1 + (bb * 112 + L) * 16, tag);
      STAMP(6);
      f4 v0 = {bhi(xg[0].x), bhi(xg[0].y), bhi(xg[0].z), bhi(xg[0].w)}, v1 = {bhi(xg[1].x), bhi(xg[1].y), bhi(xg[1].z), bhi(xg[1].w)};
      f4 t = v0 + v1;
      const float mean = wave_sum(((t.x + t.y) + (t.z + t.w)) * cnt);
      const f4 d0 = v0 - mean, d1 = v1 - mean;
      t = d0 * d0 + d1 * d1;
      const float var = rbf(wave_sum(((t.x + t.y) + (t.z + t.w)) * cnt));
      const float mb = rbf(mean);
      v0 = (v0 - mb) * sc[0]; v1 = (v1 - mb) * sc[1];
      const float inv = __builtin_amdgcn_rcpf(rbf(__builtin_amdgcn_sqrtf(rbf(var + 1e-5f))));
      v0 = v0 * inv + sh[0]; v1 = v1 * inv + sh[1];
      *(LAS v2u*)(S + ao) = v2u{pk(v0.x, v0.y), pk(v0.z, v0.w)};
      *(LAS v2u*)(S + ao + 448) = v2u{pk(v1.x, v1.y), pk(v1.z, v1.w)};
      asm volatile("s_waitcnt lgkmcnt(0)\n\ts_barrier\n\ts_barrier" ::: "memory");
      if (bb < 7) {
        const v4u h0 = *(LAS v4u*)(S + ho), h1 = *(LAS v4u*)(S + ho + 64);
        f4 d[4];
#pragma unroll
        for (int cc = 0; cc < 4; ++cc) d[cc] = mfma(f2_[4 + cc], h1, mfma(f2_[cc], h0, f4{0.f, 0.f, 0.f, 0.f}));
        const bool hi = n >> 3;
        put(ws, off, enc4(f4{hi ? d[0].y : d[0].x, hi ? d[1].y : d[1].x, hi ? d[2].y : d[2].x, hi ? d[3].y : d[3].x}, tag));
        put(ws, off + 128, enc4(f4{hi ? d[0].w : d[0].z, hi ? d[1].w : d[1].z, hi ? d[2].w : d[2].z, hi ? d[3].w : d[3].z}, tag));
      }
      STAMP(7);
    }
  } else if (j == 2 && x < 7 && w == 2) {
      const v2u xv = __builtin_amdgcn_raw_buffer_load_b64(mk(xin, 8 * D * 2), b * 896 + x * 128 + n * 8, 0, 0);
      lwait(FL + F_PV, nun);
      v4u gg[2];
      poll<2, 336 * 16, false>(gg, ws, WS_MOD + (b * 672 + 224 + x * 16 + n) * 16, tag);
      f4 xf = {blo(xv.x), bhi(xv.x), blo(xv.y), bhi(xv.y)}, gaf = {bhi(gg[0].x), bhi(gg[0].y), bhi(gg[0].z), bhi(gg[0].w)}, gbf = {bhi(gg[1].x), bhi(gg[1].y), bhi(gg[1].z), bhi(gg[1].w)};
      int o1 = lane < 16 ? WS_X1 + (b * 112 + x * 16 + n) * 16 : 0x7ffffff0, o3 = WS_S3P + ((b * 7 + x) * 448 + lane) * 16, oo = lane < 16 ? b * 896 + x * 128 + n * 8 : 0x7ffffff0;
      PIN(xf); PIN(gaf); PIN(gbf); PIN(o1); PIN(o3); PIN(oo);
      v4u p1[2];
      poll<2, 1024, false>(p1, ws, WS_S1P + ((b * 7 + x) * 128 + lane) * 16, tag);
      STAMP(1);
      f4 a = dec4(p1[0]) + dec4(p1[1]);
      f4 x1;
#pragma unroll
      for (int i = 0; i < 4; ++i) x1[i] = rbf(xf[i] + rbf(gaf[i] * rbf(rows_sum(a[i]))));
      put(ws, o1, enc4b(x1, tag));
      STAMP(5);
      v4u p3[7];
      poll<7, 1024, false>(p3, ws, o3, tag);
      STAMP(6);
      a = ((dec4(p3[0]) + dec4(p3[1])) + (dec4(p3[2]) + dec4(p3[3]))) + ((dec4(p3[4]) + dec4(p3[5])) + dec4(p3[6]));
      f4 y;
#pragma unroll
      for (int i = 0; i < 4; ++i) y[i] = x1[i] + rbf(gbf[i] * rbf(rows_sum(a[i])));
      __builtin_amdgcn_raw_buffer_store_b64(v2u{pk(y[0], y[1]), pk(y[2], y[3])}, mk(out, 8 * D * 2), oo, 0, 0);
      if (idx == 0 && lane == 0) __builtin_amdgcn_raw_buffer_store_b32(tag, ws, WS_EPOCH, 0, 16);
      STAMP(7);
  }
  if (PROBE_COND) { if (st[PROBE]) { const u64 until = st[PROBE] + 4000; while (rt() < until) __builtin_amdgcn_s_sleep(2); } }
}

#undef kc
#undef vc
#undef ws_
#undef wmod
#undef wq
#undef cin
#undef xin
#undef wo
#undef w1
#undef w3
#undef w2
#undef out
#undef tag

extern "C" void kernel_launch(void* const* d_in, const int* in_sizes, int n_in, void* d_out, int out_size, void* d_ws, size_t ws_size,
                              hipStream_t stream) {
  fused_decode<<<256, 768, 0, stream>>>((const char*)d_in[1], (const char*)d_in[2], (char*)d_ws, (const char*)d_in[10], (const char*)d_in[3],
                                        (const char*)d_in[9], (const char*)d_in[0], (const char*)d_in[4], (const char*)d_in[6],
                                        (const char*)d_in[7], (const char*)d_in[8], (char*)d_out);
}
```

```cpp
#define VLATE 0
#define NUN 0xAA57
#define STAMPS 1
#define PROBE 1
#define PROBE_COND (j == 1 && w < 4)
#include <hip/hip_runtime.h>
#include <stdint.h>

#define GAS __attribute__((address_space(1)))
#define LAS __attribute__((address_space(3)))
typedef unsigned v4u __attribute__((ext_vector_type(4)));
typedef unsigned v2u __attribute__((ext_vector_type(2)));
typedef float f4 __attribute__((ext_vector_type(4)));
typedef float f2 __attribute__((ext_vector_type(2)));
typedef __bf16 bf8 __attribute__((ext_vector_type(8)));
typedef __bf16 bf2 __attribute__((ext_vector_type(2)));
typedef short s4 __attribute__((ext_vector_type(4)));
typedef unsigned long long u64;
typedef __amdgpu_buffer_rsrc_t rsrc_t;

namespace {
constexpr int D = 448;
constexpr int WS_EPOCH = 0;
constexpr int MODS = 688, QS = 1040, S1S = 7 * 8 * 16 + 16, S3S = 7 * 28 * 16 + 16;
constexpr int WS_MOD = 4096;
constexpr int WS_Q = WS_MOD + 8 * MODS * 16;
constexpr int WS_ATT = WS_Q + 8 * QS * 16;
constexpr int WS_S1P = WS_ATT + 16 * 8 * 4 * 64 * 16;
constexpr int WS_X1 = WS_S1P + 8 * S1S * 16;
constexpr int WS_S3P = WS_X1 + 8 * 112 * 16;
constexpr int WS_END = WS_S3P + 8 * S3S * 16;
constexpr int WS_TS = 4 << 20;
constexpr int L_TILE = 0;
constexpr int L_TILB = 57344;
constexpr int L_PART = L_TILB + 65536;
constexpr int L_ACT = L_PART + 10 * 2048;
constexpr int AS = 1056;
constexpr int L_HH = L_ACT + 8 * AS;
constexpr int HS = 160;
constexpr int L_Q = L_HH + 8 * HS;
constexpr int L_P = L_Q + 1024;
constexpr int L_STAT = L_P + 10 * 128;
constexpr int L_FLAG = L_STAT + 10 * 16;
constexpr int L_DEAD = L_FLAG + 512;
constexpr int L_END = L_DEAD + 2048;
constexpr int F_SM = 0, F_PV = 16, F_Q = 32, F_TILB = 48, F_TILE = 64, F_ACT = 80, F_GO = 96, F_PUB = 112;
#define DLY_P 5
#define DLY_KV 18
#define NUNB 0xAA66
#define WO1 4
#define PFN 6
constexpr int CODE_KB = STAMPS ? 20 : 19;
constexpr int SPIN_G = 1 << 20, SPIN_L = 1 << 22;

__device__ __forceinline__ rsrc_t mk(const void* p, int n) { return __builtin_amdgcn_make_buffer_rsrc((void*)p, 0, n, 0x00020000); }
__device__ __forceinline__ float asf(unsigned u) { return __builtin_bit_cast(float, u); }
__device__ __forceinline__ unsigned asu(float f) { return __builtin_bit_cast(unsigned, f); }
__device__ __forceinline__ unsigned pk(float lo, float hi) { bf2 v = {(__bf16)lo, (__bf16)hi}; return __builtin_bit_cast(unsigned, v); }
__device__ __forceinline__ float rbf(float f) { return (float)(__bf16)f; }
__device__ __forceinline__ float blo(unsigned u) { return asf(u << 16); }
__device__ __forceinline__ float bhi(unsigned u) { return asf(u & 0xffff0000u); }
__device__ __forceinline__ f4 mfma(v4u a, v4u b, f4 c) {
  return __builtin_amdgcn_mfma_f32_16x16x32_bf16(__builtin_bit_cast(bf8, a), __builtin_bit_cast(bf8, b), c, 0, 0, 0);
}
template <int CTRL> __device__ __forceinline__ float dpp(float x) { return asf(__builtin_amdgcn_mov_dpp(asu(x), CTRL, 0xf, 0xf, true)); }
constexpr int XOR1 = 0xB1, XOR2 = 0x4E, ROR4 = 0x124, ROR8 = 0x128;
__device__ __forceinline__ float rows_sum(float x) {
  auto s = __builtin_amdgcn_permlane16_swap(asu(x), asu(x), false, false);
  x = asf(s[0]) + asf(s[1]);
  auto t = __builtin_amdgcn_permlane32_swap(asu(x), asu(x), false, false);
  return asf(t[0]) + asf(t[1]);
}
__device__ __forceinline__ float rows_max(float x) {
  auto s = __builtin_amdgcn_permlane16_swap(asu(x), asu(x), false, false);
  x = fmaxf(asf(s[0]), asf(s[1]));
  auto t = __builtin_amdgcn_permlane32_swap(asu(x), asu(x), false, false);
  return fmaxf(asf(t[0]), asf(t[1]));
}
__device__ __forceinline__ float wave_sum(float x) {
  x += dpp<XOR1>(x); x += dpp<XOR2>(x); x += dpp<ROR4>(x); x += dpp<ROR8>(x);
  return rows_sum(x);
}
#define CBAR() asm volatile("" ::: "memory")
__device__ __forceinline__ u64 rt() { u64 t; __builtin_amdgcn_sched_barrier(0); asm volatile("s_memrealtime %0\n\ts_waitcnt lgkmcnt(0)" : "=s"(t) :: "memory"); __builtin_amdgcn_sched_barrier(0); return t; }
#define STAMP(i) st[i] = rt()

__device__ __forceinline__ v4u enc4(f4 v, unsigned tag) {
  v4u r;
  r.x = __builtin_amdgcn_perm(tag, asu(v.x) + 0x80u, 0x03020104u);
  r.y = __builtin_amdgcn_perm(tag, asu(v.y) + 0x80u, 0x03020105u);
  r.z = __builtin_amdgcn_perm(tag, asu(v.z) + 0x80u, 0x03020106u);
  r.w = __builtin_amdgcn_perm(tag, asu(v.w) + 0x80u, 0x03020107u);
  return r;
}
__device__ __forceinline__ v4u enc4b(f4 v, unsigned tag) {
  return v4u{__builtin_amdgcn_perm(tag, asu(v.x), 0x03020104u), __builtin_amdgcn_perm(tag, asu(v.y), 0x03020105u),
             __builtin_amdgcn_perm(tag, asu(v.z), 0x03020106u), __builtin_amdgcn_perm(tag, asu(v.w), 0x03020107u)};
}
__device__ __forceinline__ unsigned tag4(v4u g) {
  return __builtin_amdgcn_perm(g.y, g.x, 0x0c0c0400u) | __builtin_amdgcn_perm(g.w, g.z, 0x04000c0cu);
}
__device__ __forceinline__ f4 dec4(v4u g) { return f4{asf(g.x & ~255u), asf(g.y & ~255u), asf(g.z & ~255u), asf(g.w & ~255u)}; }
__device__ __forceinline__ void put(rsrc_t ws, int off, v4u g) { __builtin_amdgcn_raw_buffer_store_b128(g, ws, off, 0, 16); }
__device__ __forceinline__ unsigned tagA(v4u g) { return __builtin_amdgcn_perm(g.w, g.y, 0x05040100u); }
__device__ __forceinline__ float lseA(v4u g) { return asf(__builtin_amdgcn_perm(g.y, g.w, 0x07060302u)); }

template <int N, int STEP, bool ATT>
__device__ __forceinline__ void poll(v4u (&x)[N], rsrc_t ws, int off, unsigned tag) {
#pragma nounroll
  for (int it = 0; it < SPIN_G; ++it) {
    CBAR();
#pragma unroll
    for (int l = 0; l < N; ++l) x[l] = __builtin_amdgcn_raw_buffer_load_b128(ws, off + l * STEP, 0, 16);
    unsigned bad = 0;
#pragma unroll
    for (int l = 0; l < N; ++l) bad |= (ATT ? tagA(x[l]) : tag4(x[l])) ^ tag;
    if (__builtin_amdgcn_ballot_w64(bad != 0) == 0) break;
  }
}
__device__ __forceinline__ void poll_att(v4u (&p)[8], rsrc_t ws, int off, unsigned tg) {
  unsigned need = 15u, left = SPIN_G, t0, t1;
  asm volatile(
      "0:\n\t"
      "s_bitcmp1_b32 %[need], 0\n\ts_cbranch_scc0 10f\n\t"
      "buffer_load_dwordx4 v[64:67], %[off], %[rs], 0 offen sc1\n\t"
      "buffer_load_dwordx4 v[80:83], %[off], %[rs], %[k4] offen sc1\n"
      "10:\n\t"
      "s_bitcmp1_b32 %[need], 1\n\ts_cbranch_scc0 11f\n\t"
      "buffer_load_dwordx4 v[68:71], %[off], %[rs], 0 offen offset:1024 sc1\n\t"
      "buffer_load_dwordx4 v[84:87], %[off], %[rs], %[k4] offen offset:1024 sc1\n"
      "11:\n\t"
      "s_bitcmp1_b32 %[need], 2\n\ts_cbranch_scc0 12f\n\t"
      "buffer_load_dwordx4 v[72:75], %[off], %[rs], 0 offen offset:2048 sc1\n\t"
      "buffer_load_dwordx4 v[88:91], %[off], %[rs], %[k4] offen offset:2048 sc1\n"
      "12:\n\t"
      "s_bitcmp1_b32 %[need], 3\n\ts_cbranch_scc0 13f\n\t"
      "buffer_load_dwordx4 v[76:79], %[off], %[rs], 0 offen offset:3072 sc1\n\t"
      "buffer_load_dwordx4 v[92:95], %[off], %[rs], %[k4] offen offset:3072 sc1\n"
      "13:\n\t"
      "s_waitcnt vmcnt(0)\n\t"
      "s_bitcmp1_b32 %[need], 0\n\ts_cbranch_scc0 20f\n\t"
      "v_perm_b32 %[t0], v67, v65, %[sel]\n\tv_perm_b32 %[t1], v83, v81, %[sel]\n\t"
      "v_bitop3_b32 %[t0], %[t0], %[t1], %[tg] bitop3:0x7e\n\tv_cmp_ne_u32 vcc, 0, %[t0]\n\t"
      "s_cbranch_vccnz 20f\n\ts_bitset0_b32 %[need], 0\n"
      "20:\n\t"
      "s_bitcmp1_b32 %[need], 1\n\ts_cbranch_scc0 21f\n\t"
      "v_perm_b32 %[t0], v71, v69, %[sel]\n\tv_perm_b32 %[t1], v87, v85, %[sel]\n\t"
      "v_bitop3_b32 %[t0], %[t0], %[t1], %[tg] bitop3:0x7e\n\tv_cmp_ne_u32 vcc, 0, %[t0]\n\t"
      "s_cbranch_vccnz 21f\n\ts_bitset0_b32 %[need], 1\n"
      "21:\n\t"
      "s_bitcmp1_b32 %[need], 2\n\ts_cbranch_scc0 22f\n\t"
      "v_perm_b32 %[t0], v75, v73, %[sel]\n\tv_perm_b32 %[t1], v91, v89, %[sel]\n\t"
      "v_bitop3_b32 %[t0], %[t0], %[t1], %[tg] bitop3:0x7e\n\tv_cmp_ne_u32 vcc, 0, %[t0]\n\t"
      "s_cbranch_vccnz 22f\n\ts_bitset0_b32 %[need], 2\n"
      "22:\n\t"
      "s_bitcmp1_b32 %[need], 3\n\ts_cbranch_scc0 23f\n\t"
      "v_perm_b32 %[t0], v79, v77, %[sel]\n\tv_perm_b32 %[t1], v95, v93, %[sel]\n\t"
      "v_bitop3_b32 %[t0], %[t0], %[t1], %[tg] bitop3:0x7e\n\tv_cmp_ne_u32 vcc, 0, %[t0]\n\t"
      "s_cbranch_vccnz 23f\n\ts_bitset0_b32 %[need], 3\n"
      "23:\n\t"
      "s_cmp_eq_u32 %[need], 0\n\ts_cbranch_scc1 3f\n\t"
      "s_sub_u32 %[left], %[left], 1\n\ts_cbranch_scc0 0b\n"
      "3:"
      : "={v[64:67]}"(p[0]), "={v[68:71]}"(p[1]), "={v[72:75]}"(p[2]), "={v[76:79]}"(p[3]), "={v[80:83]}"(p[4]), "={v[84:87]}"(p[5]),
        "={v[88:91]}"(p[6]), "={v[92:95]}"(p[7]), [need] "+s"(need), [left] "+s"(left), [t0] "=&v"(t0), [t1] "=&v"(t1), [off] "+v"(off), [tg] "+v"(tg)
      : [rs] "s"(ws), [k4] "s"(4096), [sel] "s"(0x05040100u)
      : "vcc", "scc", "memory");
}
__device__ __forceinline__ int lseen(unsigned v) { return __builtin_popcount((unsigned)__builtin_amdgcn_ballot_w64(v != 0u) & 0xffffu); }
__device__ __forceinline__ void lwait(volatile LAS unsigned* f, int want) {
  volatile LAS unsigned* p = f + (threadIdx.x & 15);
#pragma nounroll
  for (int it = 0; it < SPIN_L; ++it) {
    if (lseen(*p) >= want) break;
    __builtin_amdgcn_s_sleep(1);
  }
  CBAR();
}
__device__ __forceinline__ void lset(volatile LAS unsigned* f, int w) { CBAR(); f[w] = 1u; }
template <bool NT>
__device__ __forceinline__ void dma(rsrc_t rs, int voff, int soff, unsigned ldsaddr) {
  unsigned keep;
  if (NT)
    asm volatile("s_mov_b32 %0, m0\n\ts_mov_b32 m0, %3\n\ts_nop 0\n\tbuffer_load_dwordx4 %1, %2, %4 offen nt lds\n\ts_mov_b32 m0, %0"
                 : "=&s"(keep) : "v"(voff), "s"(rs), "s"(ldsaddr), "s"(soff) : "memory");
  else
    asm volatile("s_mov_b32 %0, m0\n\ts_mov_b32 m0, %3\n\ts_nop 0\n\tbuffer_load_dwordx4 %1, %2, %4 offen lds\n\ts_mov_b32 m0, %0"
                 : "=&s"(keep) : "v"(voff), "s"(rs), "s"(ldsaddr), "s"(soff) : "memory");
}
template <bool NT>
__device__ __forceinline__ void dma_tile(rsrc_t rs, int base, int stride, int q0, int dq, int n, unsigned lds, int dl, int lane) {
  const int rowoff = (lane >> 3) * stride;
  const int v0 = rowoff + (((lane & 7) ^ (((lane >> 4) & 1) << 1)) << 4), v1 = v0 ^ 64;
  int so = base + q0 * 8 * stride;
  unsigned la = lds;
#pragma nounroll
  for (int i = 0; i < n; ++i) {
    dma<NT>(rs, v0, so, la);
    dma<NT>(rs, v1, so + 8 * stride, la + 1024);
    so += dq * 8 * stride; la += dl;
  }
}
__device__ __forceinline__ v4u trfrag(LAS char* p) {
  s4 a = __builtin_amdgcn_ds_read_tr16_b64_v4i16((LAS s4*)p), b = __builtin_amdgcn_ds_read_tr16_b64_v4i16((LAS s4*)(p + 512));
  v2u x = __builtin_bit_cast(v2u, a), y = __builtin_bit_cast(v2u, b);
  return v4u{x.x, x.y, y.x, y.y};
}
__device__ __forceinline__ int tr_base(int lane, int nt) {
  const int g = lane >> 4, q4 = (lane & 15) >> 2, p = lane & 3;
  return (8 * g + q4) * 128 + ((nt ^ ((q4 >> 1) | ((g & 1) << 1))) << 5) + p * 8;
}
__device__ __forceinline__ void ln_norm(float (&x)[8], bool act) {
  float s = 0.f;
#pragma unroll
  for (int i = 0; i < 8; ++i) s += x[i];
  const float mean = wave_sum(act ? s : 0.f) * (1.0f / D);
  float q = 0.f;
#pragma unroll
  for (int i = 0; i < 8; ++i) { const float d = x[i] - mean; q += d * d; }
  const float var = rbf(wave_sum(act ? q : 0.f) * (1.0f / D));
  const float mb = rbf(mean);
  const float inv = __builtin_amdgcn_rcpf(rbf(__builtin_amdgcn_sqrtf(rbf(var + 1e-5f))));
#pragma unroll
  for (int i = 0; i < 8; ++i) x[i] = rbf(rbf(x[i] - mb) * inv);
}
__device__ __forceinline__ void mod_store(const float (&ln)[8], const v4u (&m)[4], LAS char* row, int L) {
  float h[8];
#pragma unroll
  for (int i = 0; i < 8; ++i) {
    const float sh = bhi(m[i >> 2][i & 3]), sc = bhi(m[2 + (i >> 2)][i & 3]);
    h[i] = rbf(ln[i] * rbf(1.0f + sc)) + sh;
  }
  *(LAS v2u*)(row + L * 8) = v2u{pk(h[0], h[1]), pk(h[2], h[3])};
  *(LAS v2u*)(row + 448 + L * 8) = v2u{pk(h[4], h[5]), pk(h[6], h[7])};
}
__device__ __forceinline__ float silu(float v) {
  const float d = 1.0f + __builtin_amdgcn_exp2f(fminf(-1.4426950408889634f * v, 126.0f)), r = __builtin_amdgcn_rcpf(d), q = v * r;
  return __builtin_fmaf(__builtin_fmaf(-q, d, v), r, q);
}
#define PIN(x) asm volatile("" : "+v"(x))
#define BARRIER() asm volatile("s_waitcnt lgkmcnt(0)\n\ts_barrier" ::: "memory")
}

typedef unsigned s16u __attribute__((ext_vector_type(16)));
typedef unsigned s8u __attribute__((ext_vector_type(8)));
#define KPTR(A, i) ((char*)(((u64)(A)[2 * (i) + 1] << 32) | (A)[2 * (i)]))
#define kc KPTR(KA, 0)
#define vc KPTR(KA, 1)
#define ws_ KPTR(KA, 2)
#define wmod KPTR(KA, 3)
#define wq KPTR(KA, 4)
#define cin KPTR(KA, 5)
#define xin KPTR(KA, 6)
#define wo KPTR(KA, 7)
#define w1 KPTR(KB, 0)
#define w3 KPTR(KB, 1)
#define w2 KPTR(KB, 2)
#define out KPTR(KB, 3)
#define KWAIT() do { asm volatile("s_waitcnt lgkmcnt(0) ; KWAIT" : "={s[72:87]}"(KA), "={s[88:95]}"(KB)); ws = mk(ws_, WS_END); \
    int zoff = WS_EPOCH; PIN(zoff); epoch = __builtin_amdgcn_raw_buffer_load_b32(ws, zoff, 0, 16); } while (0)
#define tag (epoch + 0x01010101u)

__global__ void __launch_bounds__(768, 3)
fused_decode(const char*, const char*, char*, const char*, const char*, const char*, const char*, const char*, const char*, const char*, const char*, char*) {
  __shared__ __attribute__((aligned(16))) char smem[L_END];
  LAS char* S = (LAS char*)smem;
  volatile LAS unsigned* FL = (volatile LAS unsigned*)(S + L_FLAG);
  u64 st[8] = {0, 0, 0, 0, 0, 0, 0, 0};
  STAMP(0);
  const int blk = blockIdx.x, lane = threadIdx.x & 63, w = __builtin_amdgcn_readfirstlane(threadIdx.x >> 6);
  const unsigned lb = (unsigned)(size_t)S;
  const int x = blk & 7, r = blk >> 3, b = r >> 2, j = r & 3;
  s16u KA; s8u KB;
  {
    u64 pc; unsigned keep;
    const v2u zero = {0u, 0u};
    asm volatile(
        "s_cmp_lg_u32 %[w], 0\n\ts_cbranch_scc1 1f\n\t"
        "ds_write_b64 %[fa], %[z]\n\t"
        "s_getpc_b64 %[pc]\n\ts_mov_b32 %[k], m0\n\ts_mov_b32 m0, %[dead]\n\ts_nop 0\n\t"
        "global_load_lds_dwordx4 %[co], %[pc]\n\ts_mov_b32 m0, %[k]\n\t"
        "s_waitcnt lgkmcnt(0)\n"
        "1:\n\t"
        "s_load_dwordx16 s[72:87], %[ka], 0\n\ts_load_dwordx8 s[88:95], %[ka], 64\n\ts_barrier"
        : [pc] "=&s"(pc), [k] "=&s"(keep)
        : [w] "s"(w), [fa] "v"(lb + L_FLAG + lane * 8), [z] "v"(zero), [dead] "s"(lb + L_DEAD), [co] "v"((r < CODE_KB ? CODE_KB - 1 - r : 31 - r) * 1024 + 384 + lane * 16),
          [ka] "s"(__builtin_amdgcn_kernarg_segment_ptr())
        : "scc", "memory", "s72", "s73", "s74", "s75", "s76", "s77", "s78", "s79", "s80", "s81", "s82", "s83", "s84", "s85", "s86", "s87",
          "s88", "s89", "s90", "s91", "s92", "s93", "s94", "s95");
  }
  static_assert((NUN & 15) + ((NUN >> 4) & 15) == 12 && (NUNB & 15) + ((NUNB >> 4) & 15) == 12 && (NUN >> 8) == 0xAA && (NUNB >> 8) == 0xAA &&
                (NUN & 15) <= 8 && (NUNB & 15) <= 8 && (NUN & 15) >= 4 && (NUNB & 15) >= 4, "unit split");
  const int nsel = b < 7 ? NUN : NUNB;
  const int nun = (nsel >> (4 * j)) & 15, u0 = j == 1 ? nsel & 15 : (0x160c0000 >> (8 * j)) & 255, nh = 12 - nun;
  const int idx = b * 8 + x;
  const bool urgent = x < 2 && b < 7;
  const int k0 = (x - 2) * 7 + b;
  const bool isP0 = j == 0 && (x < 2 || b == 7 || k0 < 12), isP1 = j == 1, isS1 = isP1 && b < 7;
  const bool isF = j == 0 && !isP0 && k0 < 40;
  const int fidx = k0 - 12;
  const int g = lane >> 4, n = lane & 15;
  rsrc_t ws;
  unsigned epoch;
  __builtin_amdgcn_s_setprio(3);
  const bool act = lane < 56;
  const int L = act ? lane : 55;
  const bool isP = isP0 || isP1, pf = j >= 2 && urgent && w < 2;
  const int tile = isP1 ? idx : isF ? fidx : urgent ? 2 * b + x : b == 7 ? 26 + 2 * x : 14 + k0;
  KWAIT();
  if (isP && w >= 4) dma<false>(mk(isP0 ? cin : xin, 8 * D * 2), lane * 16, (w - 4) * 896, lb + L_PART + (w - 4) * 1024);
  if (!(urgent && j != 1)) __builtin_amdgcn_s_sleep(DLY_P);
  if (((isP || isF) && w < 4) || pf) {
    const int sa = isP1 ? 8192 : isF ? 3584 : 5376;
    dma_tile<true>(mk(isP1 ? wq : isF ? w1 : wmod, D * sa), tile * 128, sa, pf ? 56 - PFN * (2 * j + w - 3) : w * 2, pf ? 2 : 8, pf ? PFN / 2 : 7,
                   lb + (pf ? L_DEAD : L_TILE + w * 2048), pf ? 0 : 8192, lane);
    if (isS1 || (j == 0 && b == 7))
      dma_tile<true>(mk(isP1 ? wo : wmod, isP1 ? 4096 * 896 : D * 5376), isP1 ? x * 512 * 896 + b * 128 : tile * 128 + 128, isP1 ? 896 : 5376,
                     w * 2, 8, isP1 ? WO1 : 7, lb + L_TILB + w * 2048, 8192, lane);
  }
  if (__builtin_expect(isP, 0)) {
    if (w < 4) {
      unsigned tb = L_TILE + tr_base(lane, w), ao = L_ACT + (n & 7) * AS + g * 16;
      int off = n >= 8 ? 0x7ffff000 : (isP0 ? WS_MOD + n * MODS * 16 : WS_Q + n * QS * 16) + (tile * 16 + w * 4 + g) * 16;
      PIN(tb); PIN(ao); PIN(off);
      v4u wf[14], bq[14];
      if (isP0) {
        lwait(FL + F_ACT, 8);
#pragma unroll
        for (int ks = 0; ks < 14; ++ks) bq[ks] = *(LAS v4u*)(S + ao + ks * 64);
      }
      asm volatile("s_waitcnt vmcnt(0)" ::: "memory");
      lset(FL + F_TILE, w);
      STAMP(1);
      lwait(FL + F_TILE, 4);
#pragma nounroll
      for (int t = (isP1 || b < 7) ? 1 : 0; t < 2; ++t) {
#pragma unroll
        for (int ks = 0; ks < 14; ++ks) wf[ks] = trfrag(S + tb + ks * 4096);
        if (!isP0) {
          lwait(FL + F_ACT, 8);
#pragma unroll
          for (int ks = 0; ks < 14; ++ks) bq[ks] = *(LAS v4u*)(S + ao + ks * 64);
        }
        __builtin_amdgcn_sched_barrier(0);
        f4 acc = {0.f, 0.f, 0.f, 0.f};
#pragma unroll
        for (int ks = 0; ks < 14; ++ks) acc = mfma(wf[ks], bq[ks], acc);
        put(ws, off, enc4b(f4{rbf(acc.x), rbf(acc.y), rbf(acc.z), rbf(acc.w)}, tag));
        tb += L_TILB; off += 256;
      }
      if (isS1 && WO1 < 8) dma_tile<true>(mk(wo, 4096 * 896), x * 512 * 896 + b * 128, 896, w * 2 + 8 * WO1, 8, 8 - WO1, lb + L_TILB + w * 2048 + WO1 * 8192, 8192, lane);
      lset(FL + F_GO, w);
      STAMP(7);
      asm volatile("s_waitcnt vmcnt(0)" ::: "memory");
      lset(FL + F_TILB, w);
    } else {
      const int row = w - 4;
      asm volatile("s_waitcnt vmcnt(0)" ::: "memory");
      const v2u a0 = *(LAS v2u*)(S + L_PART + row * 1024 + L * 8), a1 = *(LAS v2u*)(S + L_PART + row * 1024 + 448 + L * 8);
      float v[8] = {blo(a0.x), bhi(a0.x), blo(a0.y), bhi(a0.y), blo(a1.x), bhi(a1.x), blo(a1.y), bhi(a1.y)};
      LAS char* arow = S + L_ACT + row * AS;
      if (isP0) {
        *(LAS v2u*)(arow + L * 8) = v2u{pk(silu(v[0]), silu(v[1])), pk(silu(v[2]), silu(v[3]))};
        *(LAS v2u*)(arow + 448 + L * 8) = v2u{pk(silu(v[4]), silu(v[5])), pk(silu(v[6]), silu(v[7]))};
      } else {
        ln_norm(v, act);
#pragma unroll
        for (int i = 0; i < 8; ++i) PIN(v[i]);
        v4u m[4];
        lwait(FL + F_TILE, 4);
        poll<4, 896, false>(m, ws, WS_MOD + (row * MODS + L) * 16, tag);
        mod_store(v, m, arow, L);
      }
      lset(FL + F_ACT, row);
      STAMP(1);
      if (isP1 || urgent) lwait(FL + F_GO, 4);
    }
  }

  if (w >= nh) {
    const int slot = w - nh, u = u0 + slot;
    const size_t kvo = ((size_t)(b * 8 + x) * 4096 + 3072 + u * 32) * 512;
    const rsrc_t rk = mk(kc + kvo, 16384), rv = mk(vc + kvo, 16384);
    const int rot = blk * 5 + (blk >> 4) + w * 3, rk4 = rot & 15, rv4 = (rot >> 1) & 15;
    v4u ka[16], va[16];
    const int vb = g * 4096 + n * 16, vs = (rv4 & 7) * 512 + (rv4 >> 3) * 256;
    {
      if (!isP) __builtin_amdgcn_s_sleep(DLY_KV);
      __builtin_amdgcn_s_setprio(2);
      const int kb = (n >> 1) * 512 + ((n & 1) + 2 * g) * 16, ks = (rk4 >> 2) * 4096 + (rk4 & 3) * 128;
#pragma unroll
      for (int i = 0; i < 16; ++i) {
        ka[i] = __builtin_amdgcn_raw_buffer_load_b128(rk, kb, ks ^ ((i >> 2) * 4096 + (i & 3) * 128), 2);
        __builtin_amdgcn_sched_barrier(0);
      }
      __builtin_amdgcn_s_setprio(1);
#pragma unroll
      for (int i = 0; i < 16 - VLATE; ++i) {
        va[i] = __builtin_amdgcn_raw_buffer_load_b128(rv, vb, vs ^ ((i & 7) * 512 + (i >> 3) * 256), 2);
        __builtin_amdgcn_sched_barrier(0);
      }
    }
    __builtin_amdgcn_s_setprio(0);
    lwait(FL + F_Q, 1);
    v4u qf[4];
#pragma unroll
    for (int i = 0; i < 4; ++i) qf[i] = *(LAS v4u*)(S + L_Q + ((n >> 1) & 1) * 512 + ((i ^ (rk4 & 3)) * 128) + ((n & 1) + 2 * g) * 16);
    f4 sa[4];
#pragma unroll
    for (int t = 0; t < 4; ++t) {
      sa[t] = f4{0.f, 0.f, 0.f, 0.f};
#pragma unroll
      for (int i = 0; i < 4; ++i) sa[t] = mfma(ka[4 * t + i], qf[i], sa[t]);
    }
    const int c = n >> 2, par = n & 1;
    const f4 sc = c == 0 ? sa[0] : c == 1 ? sa[1] : c == 2 ? sa[2] : sa[3];
    const float own = par ? sc.w : sc.x, snd = par ? sc.y : sc.z;
    const float s2 = rbf(own + dpp<XOR1>(snd)) * (0.0625f * 1.4426950408889634f);
    float mx = fmaxf(s2, dpp<XOR1>(s2));
    mx = fmaxf(mx, dpp<ROR4>(mx)); mx = fmaxf(mx, dpp<ROR8>(mx)); mx = rows_max(mx);
    const float p = __builtin_amdgcn_exp2f(s2 - mx);
    float l = p + dpp<XOR1>(p);
    l += dpp<ROR4>(l); l += dpp<ROR8>(l); l = rows_sum(l);
    LAS char* pb = S + L_P + slot * 128;
    *(LAS __bf16*)(pb + ((n >> 1) & 1) * 64 + ((c ^ (rk4 >> 2)) * 8 + ((2 * g + par) ^ (rv4 & 7))) * 2) = (__bf16)p;
    if (lane == 0 || lane == 2) *(LAS f2*)(S + L_STAT + slot * 16 + lane * 4) = f2{mx, l};
    lset(FL + F_SM, w);
    STAMP(3);
    __builtin_amdgcn_sched_barrier(0);
#pragma unroll
    for (int i = 16 - VLATE; i < 16; ++i) {
      va[i] = __builtin_amdgcn_raw_buffer_load_b128(rv, vb, vs ^ ((i & 7) * 512 + (i >> 3) * 256), 2);
      __builtin_amdgcn_sched_barrier(0);
    }
    STAMP(2);
    const v4u pa = *(volatile LAS v4u*)(pb + (n >> 3) * 64 + g * 16);
    unsigned po = L_PART + slot * 2048 + (g >> 1) * 1024 + (rv4 >> 3) * 512 + (g & 1) * 256 + n * 16;
    PIN(po);
    v4u am[8];
#pragma unroll
    for (int cc = 0; cc < 8; ++cc) {
      const bool on = (n & 7) == cc;
      am[cc] = v4u{on ? pa.x : 0u, on ? pa.y : 0u, on ? pa.z : 0u, on ? pa.w : 0u};
      PIN(am[cc]);
    }
#pragma unroll
    for (int hs = 0; hs < 2; ++hs) {
      v4u f[8];
#pragma unroll
      for (int jj = 0; jj < 4; ++jj) {
#pragma unroll
        for (int cc = 0; cc < 8; ++cc) f[cc][jj] = __builtin_amdgcn_perm(va[8 * hs + 2 * jj + 1][cc >> 1], va[8 * hs + 2 * jj][cc >> 1], (cc & 1) ? 0x07060302u : 0x05040100u);
        if (jj < 3) __builtin_amdgcn_sched_barrier(0);
      }
      f4 o = {0.f, 0.f, 0.f, 0.f};
#pragma unroll
      for (int cc = 0; cc < 8; ++cc) o = mfma(am[cc], f[cc], o);
      *(LAS f4*)(S + (po ^ (hs * 512))) = o;
      __builtin_amdgcn_sched_barrier(0);
    }
    lset(FL + F_PV, w);
    __builtin_amdgcn_s_setprio(3);
    STAMP(4);
  } else {
    if (w == 0) {
      v4u q[2];
      poll<2, 1024, false>(q, ws, WS_Q + (b * QS + x * 128 + lane) * 16, tag);
#pragma unroll
      for (int h = 0; h < 2; ++h)
        *(LAS v2u*)(S + L_Q + h * 512 + lane * 8) = v2u{__builtin_amdgcn_perm(q[h].y, q[h].x, 0x07060302u), __builtin_amdgcn_perm(q[h].w, q[h].z, 0x07060302u)};
      lset(FL + F_Q, 0);
      STAMP(2);
    }
    if (w < 2) {
      const int h = w;
      lwait(FL + F_SM, nun);
      const bool live = n < nun;
      const f2 ml = *(volatile LAS f2*)(S + L_STAT + (live ? n : 0) * 16 + h * 8);
      float M = fmaxf(ml.x, dpp<XOR1>(ml.x));
      M = fmaxf(M, dpp<XOR2>(M)); M = fmaxf(M, dpp<ROR4>(M)); M = fmaxf(M, dpp<ROR8>(M));
      const float be = live ? __builtin_amdgcn_exp2f(ml.x - M) : 0.f;
      float Ls = be * ml.y;
      Ls += dpp<XOR1>(Ls); Ls += dpp<XOR2>(Ls); Ls += dpp<ROR4>(Ls); Ls += dpp<ROR8>(Ls);
      const float wt = be * __builtin_amdgcn_rcpf(Ls);
      const unsigned lse = asu(M + __builtin_amdgcn_logf(Ls));
      unsigned g1 = __builtin_amdgcn_perm(lse, tag, 0x07060100u), g3 = __builtin_amdgcn_perm(lse, tag, 0x05040302u);
      float wk[10];
#pragma unroll
      for (int i = 0; i < 10; ++i) wk[i] = asf(__builtin_amdgcn_readlane(asu(wt), i));
      int aoff = WS_ATT + ((((x * 8 + b) * 2 + h) * 4 + j) * 64 + lane) * 16;
      unsigned pa0 = L_PART + h * 1024 + lane * 16;
      PIN(g1); PIN(g3); PIN(aoff); PIN(pa0);
      f4 a[10];
#pragma nounroll
      for (int it = 0; it < SPIN_L; ++it) {
        const unsigned done = FL[F_PV + n];
#pragma unroll
        for (int i = 0; i < 10; ++i) a[i] = *(volatile LAS f4*)(S + pa0 + (i < 4 || i < nun ? i * 2048 : 0));
        if (lseen(done) >= nun) break;
      }
      const f4 acc = ((a[0] * wk[0] + a[1] * wk[1]) + (a[2] * wk[2] + a[3] * wk[3])) + (((a[4] * wk[4] + a[5] * wk[5]) + (a[6] * wk[6] + a[7] * wk[7])) + (a[8] * wk[8] + a[9] * wk[9]));
      put(ws, aoff, v4u{pk(acc.x, acc.y), g1, pk(acc.z, acc.w), g3});
      lset(FL + F_PUB, w);
      STAMP(4);
    }
  }

  if (isS1) {
    if (w >= 4) {
      const int bb = w - 4;
      unsigned so = L_ACT + bb * AS + (lane >> 5) * 256 + n * 16 + (g & 1) * 8;
      PIN(so);
      lwait(FL + F_PV, nun);
      v4u pq[8];
      poll_att(pq, ws, WS_ATT + ((x * 8 + bb) * 512 + lane) * 16, tag);
#pragma unroll
      for (int h = 0; h < 2; ++h) {
        const v4u* pr = pq + 4 * h;
        const float l0 = lseA(pr[0]), l1 = lseA(pr[1]), l2 = lseA(pr[2]), l3 = lseA(pr[3]);
        const float M = fmaxf(fmaxf(l0, l1), fmaxf(l2, l3));
        const float e0 = __builtin_amdgcn_exp2f(l0 - M), e1 = __builtin_amdgcn_exp2f(l1 - M), e2 = __builtin_amdgcn_exp2f(l2 - M), e3 = __builtin_amdgcn_exp2f(l3 - M);
        const float inv = __builtin_amdgcn_rcpf((e0 + e1) + (e2 + e3));
        const float o0 = (e0 * blo(pr[0].x) + e1 * blo(pr[1].x)) + (e2 * blo(pr[2].x) + e3 * blo(pr[3].x));
        const float o1 = (e0 * bhi(pr[0].x) + e1 * bhi(pr[1].x)) + (e2 * bhi(pr[2].x) + e3 * bhi(pr[3].x));
        const float o2 = (e0 * blo(pr[0].z) + e1 * blo(pr[1].z)) + (e2 * blo(pr[2].z) + e3 * blo(pr[3].z));
        const float o3 = (e0 * bhi(pr[0].z) + e1 * bhi(pr[1].z)) + (e2 * bhi(pr[2].z) + e3 * bhi(pr[3].z));
        *(LAS v2u*)(S + so + h * 512) = v2u{pk(o0 * inv, o1 * inv), pk(o2 * inv, o3 * inv)};
      }
      STAMP(5);
      BARRIER();
    } else {
      v4u wf[16];
      unsigned tb = L_TILB + tr_base(lane, w);
      PIN(tb);
      lwait(FL + F_TILB, 4);
#pragma unroll
      for (int ks = 0; ks < 16; ++ks) wf[ks] = trfrag(S + tb + ks * 4096);
      unsigned ao = L_ACT + (n & 7) * AS + g * 16;
      int off = n < 8 ? WS_S1P + (n * S1S + (b * 8 + x) * 16 + w * 4 + g) * 16 : 0x7ffffff0;
      PIN(ao); PIN(off);
      STAMP(5);
      BARRIER();
      v4u bq[16];
#pragma unroll
      for (int ks = 0; ks < 16; ++ks) bq[ks] = *(LAS v4u*)(S + ao + ks * 64);
      __builtin_amdgcn_sched_barrier(0);
      f4 a0 = {0.f, 0.f, 0.f, 0.f};
#pragma unroll
      for (int ks = 0; ks < 16; ++ks) a0 = mfma(wf[ks], bq[ks], a0);
      put(ws, off, enc4(a0, tag));
      STAMP(6);
    }
  } else if (isF) {
    if (w < 4) {
      v4u f1[14], f3[14];
      lwait(FL + F_PUB, 2);
      dma_tile<true>(mk(w3, D * 3584), fidx * 128, 3584, w * 2, 8, 7, lb + L_TILB + w * 2048, 8192, lane);
      asm volatile("s_waitcnt vmcnt(0)" ::: "memory");
      lset(FL + F_TILE, w);
      STAMP(1);
      lwait(FL + F_TILE, 4);
      unsigned tb = L_TILE + tr_base(lane, w), tb3 = tb + L_TILB;
      PIN(tb); PIN(tb3);
#pragma unroll
      for (int ks = 0; ks < 14; ++ks) { f1[ks] = trfrag(S + tb + ks * 4096); f3[ks] = trfrag(S + tb3 + ks * 4096); }
      unsigned ao = L_ACT + (n & 7) * AS + g * 16, ho = L_HH + (n & 7) * HS + w * 32 + g * 8;
      PIN(ao); PIN(ho);
      STAMP(5);
      BARRIER();
      v4u bq[14];
#pragma unroll
      for (int ks = 0; ks < 7; ++ks) bq[ks] = *(LAS v4u*)(S + ao + ks * 64);
      __builtin_amdgcn_sched_barrier(0);
      f4 ga = {0.f, 0.f, 0.f, 0.f}, ua = ga;
#pragma unroll
      for (int ks = 0; ks < 14; ++ks) {
        ga = mfma(f1[ks], bq[ks], ga); ua = mfma(f3[ks], bq[ks], ua);
        if (ks < 7) bq[ks + 7] = *(LAS v4u*)(S + ao + (ks + 7) * 64);
        __builtin_amdgcn_sched_barrier(0);
      }
      float hh[4];
#pragma unroll
      for (int i = 0; i < 4; ++i) hh[i] = rbf(silu(rbf(ga[i]))) * rbf(ua[i]);
      *(LAS v2u*)(S + ho) = v2u{pk(hh[0], hh[1]), pk(hh[2], hh[3])};
      STAMP(6);
      BARRIER();
    } else {
      const int bb = w - 4;
      lwait(FL + F_PUB, 2);
      v2u wr[16];
      const rsrc_t r2 = mk(w2, 1792 * 896);
      const int wb = (fidx * 64 + 8 * g) * 896 + (bb < 7 ? bb : 6) * 128 + (4 * (n & 3) + (n >> 2)) * 8;
#pragma unroll
      for (int i = 0; i < 16; ++i) wr[i] = __builtin_amdgcn_raw_buffer_load_b64(r2, wb + ((i >> 3) * 32 + (i & 7)) * 896, 0, 2);
      v4u m[4];
      poll<4, 896, false>(m, ws, WS_MOD + (bb * MODS + 336 + L) * 16, tag);
      v4u f2_[8];
#pragma unroll
      for (int t = 0; t < 2; ++t)
#pragma unroll
        for (int cc = 0; cc < 4; ++cc)
#pragma unroll
          for (int jj = 0; jj < 4; ++jj) f2_[t * 4 + cc][jj] = __builtin_amdgcn_perm(wr[8 * t + 2 * jj + 1][cc >> 1], wr[8 * t + 2 * jj][cc >> 1], (cc & 1) ? 0x07060302u : 0x05040100u);
#pragma unroll
      for (int i = 0; i < 8; ++i) PIN(f2_[i]);
      f4 sh[2], sc[2];
#pragma unroll
      for (int i = 0; i < 8; ++i) { sh[i >> 2][i & 3] = bhi(m[i >> 2][i & 3]); sc[i >> 2][i & 3] = rbf(1.0f + bhi(m[2 + (i >> 2)][i & 3])); }
      unsigned ao = L_ACT + bb * AS + L * 8, ho = L_HH + (n & 7) * HS + g * 16;
      float cnt = act ? 1.0f / D : 0.f;
      int off = WS_S3P + ((n & 7) * S3S + (bb * 28 + fidx) * 16 + (n >> 3) * 4 + g) * 16;
      PIN(sh[0]); PIN(sh[1]); PIN(sc[0]); PIN(sc[1]); PIN(ao); PIN(ho); PIN(off); PIN(cnt);
      lwait(FL + F_TILE, 4);
      STAMP(5);
      v4u xg[2];
      poll<2, 896, false>(xg, ws, WS_X1 + (bb * 112 + L) * 16, tag);
      STAMP(6);
      f4 v0 = {bhi(xg[0].x), bhi(xg[0].y), bhi(xg[0].z), bhi(xg[0].w)}, v1 = {bhi(xg[1].x), bhi(xg[1].y), bhi(xg[1].z), bhi(xg[1].w)};
      f4 t = v0 + v1;
      const float mean = wave_sum(((t.x + t.y) + (t.z + t.w)) * cnt);
      const f4 d0 = v0 - mean, d1 = v1 - mean;
      t = d0 * d0 + d1 * d1;
      const float var = rbf(wave_sum(((t.x + t.y) + (t.z + t.w)) * cnt));
      const float mb = rbf(mean);
      v0 = (v0 - mb) * sc[0]; v1 = (v1 - mb) * sc[1];
      const float inv = __builtin_amdgcn_rcpf(rbf(__builtin_amdgcn_sqrtf(rbf(var + 1e-5f))));
      v0 = v0 * inv + sh[0]; v1 = v1 * inv + sh[1];
      *(LAS v2u*)(S + ao) = v2u{pk(v0.x, v0.y), pk(v0.z, v0.w)};
      *(LAS v2u*)(S + ao + 448) = v2u{pk(v1.x, v1.y), pk(v1.z, v1.w)};
      asm volatile("s_waitcnt lgkmcnt(0)\n\ts_barrier\n\ts_barrier" ::: "memory");
      if (bb < 7) {
        const v4u h0 = *(LAS v4u*)(S + ho), h1 = *(LAS v4u*)(S + ho + 64);
        f4 d[4];
#pragma unroll
        for (int cc = 0; cc < 4; ++cc) d[cc] = mfma(f2_[4 + cc], h1, mfma(f2_[cc], h0, f4{0.f, 0.f, 0.f, 0.f}));
        const bool hi = n >> 3;
        put(ws, off, enc4(f4{hi ? d[0].y : d[0].x, hi ? d[1].y : d[1].x, hi ? d[2].y : d[2].x, hi ? d[3].y : d[3].x}, tag));
        put(ws, off + 128, enc4(f4{hi ? d[0].w : d[0].z, hi ? d[1].w : d[1].z, hi ? d[2].w : d[2].z, hi ? d[3].w : d[3].z}, tag));
      }
      STAMP(7);
    }
  } else if (j == 2 && x < 7 && w == 2) {
      const v2u xv = __builtin_amdgcn_raw_buffer_load_b64(mk(xin, 8 * D * 2), b * 896 + x * 128 + n * 8, 0, 0);
      lwait(FL + F_PUB, 2);
      v4u gg[2];
      poll<2, 336 * 16, false>(gg, ws, WS_MOD + (b * MODS + 224 + x * 16 + n) * 16, tag);
      f4 xf = {blo(xv.x), bhi(xv.x), blo(xv.y), bhi(xv.y)}, gaf = {bhi(gg[0].x), bhi(gg[0].y), bhi(gg[0].z), bhi(gg[0].w)}, gbf = {bhi(gg[1].x), bhi(gg[1].y), bhi(gg[1].z), bhi(gg[1].w)};
      int o1 = lane < 16 ? WS_X1 + (b * 112 + x * 16 + n) * 16 : 0x7ffffff0, o3 = WS_S3P + (b * S3S + x * 448 + lane) * 16, oo = lane < 16 ? b * 896 + x * 128 + n * 8 : 0x7ffffff0;
      PIN(xf); PIN(gaf); PIN(gbf); PIN(o1); PIN(o3); PIN(oo);
      v4u p1[2];
      poll<2, 1024, false>(p1, ws, WS_S1P + (b * S1S + x * 128 + lane) * 16, tag);
      STAMP(1);
      f4 a = dec4(p1[0]) + dec4(p1[1]);
      f4 x1;
#pragma unroll
      for (int i = 0; i < 4; ++i) x1[i] = rbf(xf[i] + rbf(gaf[i] * rbf(rows_sum(a[i]))));
      put(ws, o1, enc4b(x1, tag));
      STAMP(5);
      v4u p3[7];
      poll<7, 1024, false>(p3, ws, o3, tag);
      STAMP(6);
      a = ((dec4(p3[0]) + dec4(p3[1])) + (dec4(p3[2]) + dec4(p3[3]))) + ((dec4(p3[4]) + dec4(p3[5])) + dec4(p3[6]));
      f4 y;
#pragma unroll
      for (int i = 0; i < 4; ++i) y[i] = x1[i] + rbf(gbf[i] * rbf(rows_sum(a[i])));
      __builtin_amdgcn_raw_buffer_store_b64(v2u{pk(y[0], y[1]), pk(y[2], y[3])}, mk(out, 8 * D * 2), oo, 0, 0);
      if (idx == 0 && lane == 0) __builtin_amdgcn_raw_buffer_store_b32(tag, ws, WS_EPOCH, 0, 16);
      STAMP(7);
  }
  if (PROBE_COND) { if (st[PROBE]) { const u64 until = st[PROBE] + 4000; while (rt() < until) __builtin_amdgcn_s_sleep(2); } }
}

#undef kc
#undef vc
#undef ws_
#undef wmod
#undef wq
#undef cin
#undef xin
#undef wo
#undef w1
#undef w3
#undef w2
#undef out
#undef tag

extern "C" void kernel_launch(void* const* d_in, const int* in_sizes, int n_in, void* d_out, int out_size, void* d_ws, size_t ws_size,
                              hipStream_t stream) {
  fused_decode<<<256, 768, 0, stream>>>((const char*)d_in[1], (const char*)d_in[2], (char*)d_ws, (const char*)d_in[10], (const char*)d_in[3],
                                        (const char*)d_in[9], (const char*)d_in[0], (const char*)d_in[4], (const char*)d_in[6],
                                        (const char*)d_in[7], (const char*)d_in[8], (char*)d_out);
}
```

```cpp
#include <hip/hip_runtime.h>
#include <stdint.h>

#define GAS __attribute__((address_space(1)))
#define LAS __attribute__((address_space(3)))
typedef unsigned v4u __attribute__((ext_vector_type(4)));
typedef unsigned v2u __attribute__((ext_vector_type(2)));
typedef float f4 __attribute__((ext_vector_type(4)));
typedef float f2 __attribute__((ext_vector_type(2)));
typedef __bf16 bf8 __attribute__((ext_vector_type(8)));
typedef __bf16 bf2 __attribute__((ext_vector_type(2)));
typedef short s4 __attribute__((ext_vector_type(4)));
typedef unsigned long long u64;
typedef __amdgpu_buffer_rsrc_t rsrc_t;

#define STAMPS 0

namespace {
constexpr int D = 448;
constexpr int WS_EPOCH = 0;
constexpr int MODS = 688, QS = 1040, S1S = 7 * 8 * 16 + 16, S3S = 7 * 28 * 16 + 16;
constexpr int WS_MOD = 4096;
constexpr int WS_Q = WS_MOD + 8 * MODS * 16;
constexpr int WS_ATT = WS_Q + 8 * QS * 16;
constexpr int WS_S1P = WS_ATT + 16 * 8 * 4 * 64 * 16;
constexpr int WS_X1 = WS_S1P + 8 * S1S * 16;
constexpr int WS_S3P = WS_X1 + 8 * 112 * 16;
constexpr int WS_END = WS_S3P + 8 * S3S * 16;
constexpr int WS_TS = 4 << 20;
constexpr int L_TILE = 0;
constexpr int L_TILB = 57344;
constexpr int L_PART = L_TILB + 65536;
constexpr int L_ACT = L_PART + 10 * 2048;
constexpr int AS = 1056;
constexpr int L_HH = L_ACT + 8 * AS;
constexpr int HS = 160;
constexpr int L_Q = L_HH + 8 * HS;
constexpr int L_P = L_Q + 1024;
constexpr int L_STAT = L_P + 10 * 128;
constexpr int L_FLAG = L_STAT + 10 * 16;
constexpr int L_DEAD = L_FLAG + 512;
constexpr int L_END = L_DEAD + 2048;
constexpr int F_SM = 0, F_PV = 16, F_Q = 32, F_TILB = 48, F_TILE = 64, F_ACT = 80, F_GO = 96, F_PUB = 112;
#define DLY_P 5
#define DLY_KV 18
#define NUN 0xAA48
#define NUNB 0xAA66
#define VLATE 0
#define ASLP 2
#define ADIR 1
#define PFN 6
constexpr int CODE_KB = STAMPS ? 21 : 20;
constexpr int SPIN_G = 1 << 20, SPIN_L = 1 << 22;

__device__ __forceinline__ rsrc_t mk(const void* p, int n) { return __builtin_amdgcn_make_buffer_rsrc((void*)p, 0, n, 0x00020000); }
__device__ __forceinline__ float asf(unsigned u) { return __builtin_bit_cast(float, u); }
__device__ __forceinline__ unsigned asu(float f) { return __builtin_bit_cast(unsigned, f); }
__device__ __forceinline__ unsigned pk(float lo, float hi) { bf2 v = {(__bf16)lo, (__bf16)hi}; return __builtin_bit_cast(unsigned, v); }
__device__ __forceinline__ float rbf(float f) { return (float)(__bf16)f; }
__device__ __forceinline__ float blo(unsigned u) { return asf(u << 16); }
__device__ __forceinline__ float bhi(unsigned u) { return asf(u & 0xffff0000u); }
__device__ __forceinline__ f4 mfma(v4u a, v4u b, f4 c) {
  return __builtin_amdgcn_mfma_f32_16x16x32_bf16(__builtin_bit_cast(bf8, a), __builtin_bit_cast(bf8, b), c, 0, 0, 0);
}
template <int CTRL> __device__ __forceinline__ float dpp(float x) { return asf(__builtin_amdgcn_mov_dpp(asu(x), CTRL, 0xf, 0xf, true)); }
constexpr int XOR1 = 0xB1, XOR2 = 0x4E, ROR4 = 0x124, ROR8 = 0x128;
__device__ __forceinline__ float rows_sum(float x) {
  auto s = __builtin_amdgcn_permlane16_swap(asu(x), asu(x), false, false);
  x = asf(s[0]) + asf(s[1]);
  auto t = __builtin_amdgcn_permlane32_swap(asu(x), asu(x), false, false);
  return asf(t[0]) + asf(t[1]);
}
__device__ __forceinline__ float rows_max(float x) {
  auto s = __builtin_amdgcn_permlane16_swap(asu(x), asu(x), false, false);
  x = fmaxf(asf(s[0]), asf(s[1]));
  auto t = __builtin_amdgcn_permlane32_swap(asu(x), asu(x), false, false);
  return fmaxf(asf(t[0]), asf(t[1]));
}
__device__ __forceinline__ float wave_sum(float x) {
  x += dpp<XOR1>(x); x += dpp<XOR2>(x); x += dpp<ROR4>(x); x += dpp<ROR8>(x);
  return rows_sum(x);
}
#define CBAR() asm volatile("" ::: "memory")
__device__ __forceinline__ u64 rt() { u64 t; __builtin_amdgcn_sched_barrier(0); asm volatile("s_memrealtime %0\n\ts_waitcnt lgkmcnt(0)" : "=s"(t) :: "memory"); __builtin_amdgcn_sched_barrier(0); return t; }
#define STAMP(i)

__device__ __forceinline__ v4u enc4(f4 v, unsigned tag) {
  v4u r;
  r.x = __builtin_amdgcn_perm(tag, asu(v.x) + 0x80u, 0x03020104u);
  r.y = __builtin_amdgcn_perm(tag, asu(v.y) + 0x80u, 0x03020105u);
  r.z = __builtin_amdgcn_perm(tag, asu(v.z) + 0x80u, 0x03020106u);
  r.w = __builtin_amdgcn_perm(tag, asu(v.w) + 0x80u, 0x03020107u);
  return r;
}
__device__ __forceinline__ v4u enc4b(f4 v, unsigned tag) {
  return v4u{__builtin_amdgcn_perm(tag, asu(v.x), 0x03020104u), __builtin_amdgcn_perm(tag, asu(v.y), 0x03020105u),
             __builtin_amdgcn_perm(tag, asu(v.z), 0x03020106u), __builtin_amdgcn_perm(tag, asu(v.w), 0x03020107u)};
}
__device__ __forceinline__ unsigned tag4(v4u g) {
  return __builtin_amdgcn_perm(g.y, g.x, 0x0c0c0400u) | __builtin_amdgcn_perm(g.w, g.z, 0x04000c0cu);
}
__device__ __forceinline__ f4 dec4(v4u g) { return f4{asf(g.x & ~255u), asf(g.y & ~255u), asf(g.z & ~255u), asf(g.w & ~255u)}; }
__device__ __forceinline__ void put(rsrc_t ws, int off, v4u g) { __builtin_amdgcn_raw_buffer_store_b128(g, ws, off, 0, 16); }
__device__ __forceinline__ unsigned tagA(v4u g) { return __builtin_amdgcn_perm(g.w, g.y, 0x05040100u); }
__device__ __forceinline__ float lseA(v4u g) { return asf(__builtin_amdgcn_perm(g.y, g.w, 0x07060302u)); }

template <int N, int STEP, bool ATT>
__device__ __forceinline__ void poll(v4u (&x)[N], rsrc_t ws, int off, unsigned tag) {
#pragma nounroll
  for (int it = 0; it < SPIN_G; ++it) {
    CBAR();
#pragma unroll
    for (int l = 0; l < N; ++l) x[l] = __builtin_amdgcn_raw_buffer_load_b128(ws, off + l * STEP, 0, 16);
    unsigned bad = 0;
#pragma unroll
    for (int l = 0; l < N; ++l) bad |= (ATT ? tagA(x[l]) : tag4(x[l])) ^ tag;
    if (__builtin_amdgcn_ballot_w64(bad != 0) == 0) break;
  }
}
__device__ __forceinline__ void poll_att(v4u (&p)[8], rsrc_t ws, int off, int poff, unsigned tg) {
  unsigned need = 15u, left = SPIN_G, tr, t0, t1;
  asm volatile(
      "0:\n\t"
      "s_mov_b32 %[tr], %[need]\n\ts_bcnt1_i32_b32 vcc_lo, %[need]\n\ts_cmp_le_u32 vcc_lo, %[dir]\n\ts_cbranch_scc1 4f\n\t"
      "buffer_load_dwordx4 v[96:99], %[poff], %[rs], 0 offen sc1\n\ts_waitcnt vmcnt(0)\n\t"
      "v_perm_b32 %[t0], v99, v97, %[sel]\n\tv_cmp_eq_u32 vcc, %[t0], %[tg]\n\ts_nop 0\n\t"
      "s_lshr_b32 vcc_hi, vcc_lo, 4\n\ts_and_b32 vcc_lo, vcc_lo, vcc_hi\n\ts_and_b32 %[tr], vcc_lo, %[need]\n\ts_cbranch_scc0 5f\n"
      "4:\n\t"
      "s_bitcmp1_b32 %[tr], 0\n\ts_cbranch_scc0 10f\n\t"
      "buffer_load_dwordx4 v[64:67], %[off], %[rs], 0 offen sc1\n\t"
      "buffer_load_dwordx4 v[80:83], %[off], %[rs], %[k4] offen sc1\n"
      "10:\n\t"
      "s_bitcmp1_b32 %[tr], 1\n\ts_cbranch_scc0 11f\n\t"
      "buffer_load_dwordx4 v[68:71], %[off], %[rs], 0 offen offset:1024 sc1\n\t"
      "buffer_load_dwordx4 v[84:87], %[off], %[rs], %[k4] offen offset:1024 sc1\n"
      "11:\n\t"
      "s_bitcmp1_b32 %[tr], 2\n\ts_cbranch_scc0 12f\n\t"
      "buffer_load_dwordx4 v[72:75], %[off], %[rs], 0 offen offset:2048 sc1\n\t"
      "buffer_load_dwordx4 v[88:91], %[off], %[rs], %[k4] offen offset:2048 sc1\n"
      "12:\n\t"
      "s_bitcmp1_b32 %[tr], 3\n\ts_cbranch_scc0 13f\n\t"
      "buffer_load_dwordx4 v[76:79], %[off], %[rs], 0 offen offset:3072 sc1\n\t"
      "buffer_load_dwordx4 v[92:95], %[off], %[rs], %[k4] offen offset:3072 sc1\n"
      "13:\n\t"
      "s_waitcnt vmcnt(0)\n\t"
      "s_bitcmp1_b32 %[tr], 0\n\ts_cbranch_scc0 20f\n\t"
      "v_perm_b32 %[t0], v67, v65, %[sel]\n\tv_perm_b32 %[t1], v83, v81, %[sel]\n\t"
      "v_bitop3_b32 %[t0], %[t0], %[t1], %[tg] bitop3:0x7e\n\tv_cmp_ne_u32 vcc, 0, %[t0]\n\t"
      "s_cbranch_vccnz 20f\n\ts_bitset0_b32 %[need], 0\n"
      "20:\n\t"
      "s_bitcmp1_b32 %[tr], 1\n\ts_cbranch_scc0 21f\n\t"
      "v_perm_b32 %[t0], v71, v69, %[sel]\n\tv_perm_b32 %[t1], v87, v85, %[sel]\n\t"
      "v_bitop3_b32 %[t0], %[t0], %[t1], %[tg] bitop3:0x7e\n\tv_cmp_ne_u32 vcc, 0, %[t0]\n\t"
      "s_cbranch_vccnz 21f\n\ts_bitset0_b32 %[need], 1\n"
      "21:\n\t"
      "s_bitcmp1_b32 %[tr], 2\n\ts_cbranch_scc0 22f\n\t"
      "v_perm_b32 %[t0], v75, v73, %[sel]\n\tv_perm_b32 %[t1], v91, v89, %[sel]\n\t"
      "v_bitop3_b32 %[t0], %[t0], %[t1], %[tg] bitop3:0x7e\n\tv_cmp_ne_u32 vcc, 0, %[t0]\n\t"
      "s_cbranch_vccnz 22f\n\ts_bitset0_b32 %[need], 2\n"
      "22:\n\t"
      "s_bitcmp1_b32 %[tr], 3\n\ts_cbranch_scc0 23f\n\t"
      "v_perm_b32 %[t0], v79, v77, %[sel]\n\tv_perm_b32 %[t1], v95, v93, %[sel]\n\t"
      "v_bitop3_b32 %[t0], %[t0], %[t1], %[tg] bitop3:0x7e\n\tv_cmp_ne_u32 vcc, 0, %[t0]\n\t"
      "s_cbranch_vccnz 23f\n\ts_bitset0_b32 %[need], 3\n"
      "23:\n\t"
      "s_cmp_eq_u32 %[need], 0\n\ts_cbranch_scc1 3f\n"
      "5:\n\t"
      "s_sleep %c[slp]\n\ts_sub_u32 %[left], %[left], 1\n\ts_cbranch_scc0 0b\n"
      "3:"
      : "={v[64:67]}"(p[0]), "={v[68:71]}"(p[1]), "={v[72:75]}"(p[2]), "={v[76:79]}"(p[3]), "={v[80:83]}"(p[4]), "={v[84:87]}"(p[5]),
        "={v[88:91]}"(p[6]), "={v[92:95]}"(p[7]), [need] "+s"(need), [left] "+s"(left), [tr] "=&s"(tr), [t0] "=&v"(t0), [t1] "=&v"(t1),
        [off] "+v"(off), [poff] "+v"(poff), [tg] "+v"(tg)
      : [rs] "s"(ws), [k4] "s"(4096), [sel] "s"(0x05040100u), [slp] "i"(ASLP), [dir] "i"(ADIR)
      : "vcc", "scc", "memory", "v96", "v97", "v98", "v99");
}
__device__ __forceinline__ int lseen(unsigned v) { return __builtin_popcount((unsigned)__builtin_amdgcn_ballot_w64(v != 0u) & 0xffffu); }
__device__ __forceinline__ void lwait(volatile LAS unsigned* f, int want) {
  volatile LAS unsigned* p = f + (threadIdx.x & 15);
#pragma nounroll
  for (int it = 0; it < SPIN_L; ++it) {
    if (lseen(*p) >= want) break;
    __builtin_amdgcn_s_sleep(1);
  }
  CBAR();
}
__device__ __forceinline__ void lwait4(volatile LAS unsigned* f) {
  volatile LAS unsigned* p = f + (threadIdx.x & 3);
#pragma nounroll
  for (int it = 0; it < SPIN_L; ++it) {
    if (__builtin_amdgcn_ballot_w64(*p == 0u) == 0) break;
    __builtin_amdgcn_s_sleep(1);
  }
  CBAR();
}
__device__ __forceinline__ void lset(volatile LAS unsigned* f, int w) { CBAR(); f[w] = 1u; }
template <bool NT>
__device__ __forceinline__ void dma(rsrc_t rs, int voff, int soff, unsigned ldsaddr) {
  unsigned keep;
  if (NT)
    asm volatile("s_mov_b32 %0, m0\n\ts_mov_b32 m0, %3\n\ts_nop 0\n\tbuffer_load_dwordx4 %1, %2, %4 offen nt lds\n\ts_mov_b32 m0, %0"
                 : "=&s"(keep) : "v"(voff), "s"(rs), "s"(ldsaddr), "s"(soff) : "memory");
  else
    asm volatile("s_mov_b32 %0, m0\n\ts_mov_b32 m0, %3\n\ts_nop 0\n\tbuffer_load_dwordx4 %1, %2, %4 offen lds\n\ts_mov_b32 m0, %0"
                 : "=&s"(keep) : "v"(voff), "s"(rs), "s"(ldsaddr), "s"(soff) : "memory");
}
template <bool NT>
__device__ __forceinline__ void dma_tile(rsrc_t rs, int base, int stride, int q0, int dq, int n, unsigned lds, int dl, int lane) {
  const int rowoff = (lane >> 3) * stride;
  const int v0 = rowoff + (((lane & 7) ^ (((lane >> 4) & 1) << 1)) << 4), v1 = v0 ^ 64;
  int so = base + q0 * 8 * stride;
  unsigned la = lds;
#pragma nounroll
  for (int i = 0; i < n; ++i) {
    dma<NT>(rs, v0, so, la);
    dma<NT>(rs, v1, so + 8 * stride, la + 1024);
    so += dq * 8 * stride; la += dl;
  }
}
__device__ __forceinline__ v4u trfrag(LAS char* p) {
  s4 a = __builtin_amdgcn_ds_read_tr16_b64_v4i16((LAS s4*)p), b = __builtin_amdgcn_ds_read_tr16_b64_v4i16((LAS s4*)(p + 512));
  v2u x = __builtin_bit_cast(v2u, a), y = __builtin_bit_cast(v2u, b);
  return v4u{x.x, x.y, y.x, y.y};
}
__device__ __forceinline__ int tr_base(int lane, int nt) {
  const int g = lane >> 4, q4 = (lane & 15) >> 2, p = lane & 3;
  return (8 * g + q4) * 128 + ((nt ^ ((q4 >> 1) | ((g & 1) << 1))) << 5) + p * 8;
}
__device__ __forceinline__ void ln_norm(float (&x)[8], bool act) {
  float s = 0.f;
#pragma unroll
  for (int i = 0; i < 8; ++i) s += x[i];
  const float mean = wave_sum(act ? s : 0.f) * (1.0f / D);
  float q = 0.f;
#pragma unroll
  for (int i = 0; i < 8; ++i) { const float d = x[i] - mean; q += d * d; }
  const float var = rbf(wave_sum(act ? q : 0.f) * (1.0f / D));
  const float mb = rbf(mean);
  const float inv = __builtin_amdgcn_rcpf(rbf(__builtin_amdgcn_sqrtf(rbf(var + 1e-5f))));
#pragma unroll
  for (int i = 0; i < 8; ++i) x[i] = rbf(rbf(x[i] - mb) * inv);
}
__device__ __forceinline__ void mod_store(const float (&ln)[8], const v4u (&m)[4], LAS char* row, int L) {
  float h[8];
#pragma unroll
  for (int i = 0; i < 8; ++i) {
    const float sh = bhi(m[i >> 2][i & 3]), sc = bhi(m[2 + (i >> 2)][i & 3]);
    h[i] = rbf(ln[i] * rbf(1.0f + sc)) + sh;
  }
  *(LAS v2u*)(row + L * 8) = v2u{pk(h[0], h[1]), pk(h[2], h[3])};
  *(LAS v2u*)(row + 448 + L * 8) = v2u{pk(h[4], h[5]), pk(h[6], h[7])};
}
__device__ __forceinline__ float silu(float v) {
  const float d = 1.0f + __builtin_amdgcn_exp2f(fminf(-1.4426950408889634f * v, 126.0f)), r = __builtin_amdgcn_rcpf(d), q = v * r;
  return __builtin_fmaf(__builtin_fmaf(-q, d, v), r, q);
}
#define PIN(x) asm volatile("" : "+v"(x))
#define BARRIER() asm volatile("s_waitcnt lgkmcnt(0)\n\ts_barrier" ::: "memory")
}

typedef unsigned s16u __attribute__((ext_vector_type(16)));
typedef unsigned s8u __attribute__((ext_vector_type(8)));
#define KPTR(A, i) ((char*)(((u64)(A)[2 * (i) + 1] << 32) | (A)[2 * (i)]))
#define kc KPTR(KA, 0)
#define vc KPTR(KA, 1)
#define ws_ KPTR(KA, 2)
#define wmod KPTR(KA, 3)
#define wq KPTR(KA, 4)
#define cin KPTR(KA, 5)
#define xin KPTR(KA, 6)
#define wo KPTR(KA, 7)
#define w1 KPTR(KB, 0)
#define w3 KPTR(KB, 1)
#define w2 KPTR(KB, 2)
#define out KPTR(KB, 3)
#define KWAIT() do { asm volatile("s_waitcnt lgkmcnt(0) ; KWAIT" : "={s[72:87]}"(KA), "={s[88:95]}"(KB)); ws = mk(ws_, WS_END); \
    int zoff = WS_EPOCH; PIN(zoff); epoch = __builtin_amdgcn_raw_buffer_load_b32(ws, zoff, 0, 16); } while (0)
#define tag (epoch + 0x01010101u)

__global__ void __launch_bounds__(768, 3)
fused_decode(const char*, const char*, char*, const char*, const char*, const char*, const char*, const char*, const char*, const char*, const char*, char*) {
  __shared__ __attribute__((aligned(16))) char smem[L_END];
  LAS char* S = (LAS char*)smem;
  volatile LAS unsigned* FL = (volatile LAS unsigned*)(S + L_FLAG);
  STAMP(0);
  const int blk = blockIdx.x, lane = threadIdx.x & 63, w = __builtin_amdgcn_readfirstlane(threadIdx.x >> 6);
  const unsigned lb = (unsigned)(size_t)S;
  const int x = blk & 7, r = blk >> 3, b = r >> 2, j = r & 3;
  s16u KA; s8u KB;
  {
    u64 pc; unsigned keep;
    const v2u zero = {0u, 0u};
    asm volatile(
        "s_cmp_lg_u32 %[w], 0\n\ts_cbranch_scc1 1f\n\t"
        "ds_write_b64 %[fa], %[z]\n\t"
        "s_getpc_b64 %[pc]\n\ts_mov_b32 %[k], m0\n\ts_mov_b32 m0, %[dead]\n\ts_nop 0\n\t"
        "global_load_lds_dwordx4 %[co], %[pc]\n\ts_mov_b32 m0, %[k]\n\t"
        "s_waitcnt lgkmcnt(0)\n"
        "1:\n\t"
        "s_load_dwordx16 s[72:87], %[ka], 0\n\ts_load_dwordx8 s[88:95], %[ka], 64\n\ts_barrier"
        : [pc] "=&s"(pc), [k] "=&s"(keep)
        : [w] "s"(w), [fa] "v"(lb + L_FLAG + lane * 8), [z] "v"(zero), [dead] "s"(lb + L_DEAD), [co] "v"((r < CODE_KB ? CODE_KB - 1 - r : 31 - r) * 1024 + 384 + lane * 16),
          [ka] "s"(__builtin_amdgcn_kernarg_segment_ptr())
        : "scc", "memory", "s72", "s73", "s74", "s75", "s76", "s77", "s78", "s79", "s80", "s81", "s82", "s83", "s84", "s85", "s86", "s87",
          "s88", "s89", "s90", "s91", "s92", "s93", "s94", "s95");
  }
  static_assert((NUN & 15) + ((NUN >> 4) & 15) == 12 && (NUNB & 15) + ((NUNB >> 4) & 15) == 12 && (NUN >> 8) == 0xAA && (NUNB >> 8) == 0xAA &&
                (NUN & 15) <= 8 && (NUNB & 15) <= 8 && (NUN & 15) >= 4 && (NUNB & 15) >= 4, "unit split");
  const int nsel = b < 7 ? NUN : NUNB;
  const int nun = (nsel >> (4 * j)) & 15, u0 = j == 1 ? nsel & 15 : (0x160c0000 >> (8 * j)) & 255, nh = 12 - nun;
  const int idx = b * 8 + x;
  const bool urgent = x < 2 && b < 7;
  const int k0 = (x - 2) * 7 + b;
  const bool isP0 = j == 0 && (x < 2 || b == 7 || k0 < 12), isP1 = j == 1, isS1 = isP1 && b < 7;
  const bool isF = j == 0 && !isP0 && k0 < 40;
  const int fidx = k0 - 12;
  const int g = lane >> 4, n = lane & 15;
  rsrc_t ws;
  unsigned epoch;
  __builtin_amdgcn_s_setprio(3);
  const bool act = lane < 56;
  const int L = act ? lane : 55;
  const bool isP = isP0 || isP1, pf = j >= 2 && urgent && w < 2;
  const int tile = isP1 ? idx : isF ? fidx : urgent ? 2 * b + x : b == 7 ? 26 + 2 * x : 14 + k0;
  const bool stager = isP1 ? w < 8 : isP0 && w >= 4;
  const int row = isP1 ? w : w - 4;
  KWAIT();
  if (stager) dma<false>(mk(isP0 ? cin : xin, 8 * D * 2), lane * 16, row * 896, lb + L_PART + row * 1024);
  if (!(urgent && j != 1)) __builtin_amdgcn_s_sleep(DLY_P);
  if (((isP || isF) && w < 4) || pf) {
    const int sa = isP1 ? 8192 : isF ? 3584 : 5376;
    const rsrc_t rt_ = mk(isP1 ? wq : isF ? w1 : wmod, D * sa);
    dma_tile<true>(rt_, tile * 128, sa, pf ? 56 - PFN * (2 * j + w - 3) : w * 2, pf ? 2 : 8, pf ? PFN / 2 : 7,
                   lb + (pf ? L_DEAD : L_TILE + w * 2048), pf ? 0 : 8192, lane);
    if (j == 0 && b == 7) dma_tile<true>(rt_, tile * 128 + 128, 5376, w * 2, 8, 7, lb + L_TILB + w * 2048, 8192, lane);
  }
  if (__builtin_expect(isP0 || stager, 0)) {
    unsigned tb = L_TILE + tr_base(lane, w), ao = L_ACT + (n & 7) * AS + g * 16;
    int off = n >= 8 ? 0x7ffff000 : (isP0 ? WS_MOD + n * MODS * 16 : WS_Q + n * QS * 16) + (tile * 16 + w * 4 + g) * 16;
    PIN(tb); PIN(ao); PIN(off);
    v4u wf[14], bq[14];
    if (!stager) {
      lwait(FL + F_ACT, 8);
#pragma unroll
      for (int ks = 0; ks < 14; ++ks) bq[ks] = *(LAS v4u*)(S + ao + ks * 64);
      asm volatile("s_waitcnt vmcnt(0)" ::: "memory");
      lset(FL + F_TILE, w);
      STAMP(1);
      lwait(FL + F_TILE, 4);
#pragma nounroll
      for (int t = b < 7 ? 1 : 0; t < 2; ++t) {
#pragma unroll
        for (int ks = 0; ks < 14; ++ks) wf[ks] = trfrag(S + tb + ks * 4096);
        __builtin_amdgcn_sched_barrier(0);
        f4 acc = {0.f, 0.f, 0.f, 0.f};
#pragma unroll
        for (int ks = 0; ks < 14; ++ks) acc = mfma(wf[ks], bq[ks], acc);
        put(ws, off, enc4b(f4{rbf(acc.x), rbf(acc.y), rbf(acc.z), rbf(acc.w)}, tag));
        tb += L_TILB; off += 256;
      }
      lset(FL + F_GO, w);
      STAMP(7);
    } else {
      asm volatile("s_waitcnt vmcnt(0)" ::: "memory");
      if (w < 4) {
        lset(FL + F_TILE, w);
        STAMP(1);
        lwait4(FL + F_TILE);
#pragma unroll
        for (int ks = 0; ks < 14; ++ks) wf[ks] = trfrag(S + tb + ks * 4096);
      }
      const v2u a0 = *(LAS v2u*)(S + L_PART + row * 1024 + L * 8), a1 = *(LAS v2u*)(S + L_PART + row * 1024 + 448 + L * 8);
      float v[8] = {blo(a0.x), bhi(a0.x), blo(a0.y), bhi(a0.y), blo(a1.x), bhi(a1.x), blo(a1.y), bhi(a1.y)};
      LAS char* arow = S + L_ACT + row * AS;
      if (isP0) {
        *(LAS v2u*)(arow + L * 8) = v2u{pk(silu(v[0]), silu(v[1])), pk(silu(v[2]), silu(v[3]))};
        *(LAS v2u*)(arow + 448 + L * 8) = v2u{pk(silu(v[4]), silu(v[5])), pk(silu(v[6]), silu(v[7]))};
      } else {
        ln_norm(v, act);
#pragma unroll
        for (int i = 0; i < 8; ++i) PIN(v[i]);
        v4u m[4];
        lwait(FL + F_TILE, 8);
        poll<4, 896, false>(m, ws, WS_MOD + (row * MODS + L) * 16, tag);
        mod_store(v, m, arow, L);
      }
      lset(FL + F_ACT, row);
      if (w < 4) {
        lwait(FL + F_ACT, 8);
#pragma unroll
        for (int ks = 0; ks < 14; ++ks) bq[ks] = *(LAS v4u*)(S + ao + ks * 64);
        __builtin_amdgcn_sched_barrier(0);
        f4 acc = {0.f, 0.f, 0.f, 0.f};
#pragma unroll
        for (int ks = 0; ks < 14; ++ks) acc = mfma(wf[ks], bq[ks], acc);
        put(ws, off, enc4b(f4{rbf(acc.x), rbf(acc.y), rbf(acc.z), rbf(acc.w)}, tag));
        lset(FL + F_GO, w);
        STAMP(7);
        if (isS1 && w >= 2) {
          lwait(FL + F_GO, 4);
          dma_tile<true>(mk(wo, 4096 * 896), x * 512 * 896 + b * 128, 896, (w - 2) * 2, 4, 16, lb + L_TILB + (w - 2) * 2048, 4096, lane);
          asm volatile("s_waitcnt vmcnt(0)" ::: "memory");
          lset(FL + F_TILB, w);
        }
      } else {
        STAMP(1);
        if (isP1 || urgent) lwait(FL + F_GO, 4);
      }
    }
  }

  if (w >= nh) {
    const int slot = w - nh, u = u0 + slot;
    const size_t kvo = ((size_t)(b * 8 + x) * 4096 + 3072 + u * 32) * 512;
    const rsrc_t rk = mk(kc + kvo, 16384), rv = mk(vc + kvo, 16384);
    const int rot = blk * 5 + (blk >> 4) + w * 3, rk4 = rot & 15, rv4 = (rot >> 1) & 15;
    v4u ka[16], va[16];
    const int vb = g * 4096 + n * 16, vs = (rv4 & 7) * 512 + (rv4 >> 3) * 256;
    {
      if (!isP0) __builtin_amdgcn_s_sleep(DLY_KV);
      __builtin_amdgcn_s_setprio(2);
      const int kb = (n >> 1) * 512 + ((n & 1) + 2 * g) * 16, ks = (rk4 >> 2) * 4096 + (rk4 & 3) * 128;
#pragma unroll
      for (int i = 0; i < 16; ++i) {
        ka[i] = __builtin_amdgcn_raw_buffer_load_b128(rk, kb, ks ^ ((i >> 2) * 4096 + (i & 3) * 128), 2);
        __builtin_amdgcn_sched_barrier(0);
      }
      __builtin_amdgcn_s_setprio(1);
      if (isP1) {
        asm volatile("s_waitcnt vmcnt(0)" ::: "memory");
        lset(FL + F_TILE, w);
        lwait(FL + F_GO, 4);
      }
#pragma unroll
      for (int i = 0; i < 16 - VLATE; ++i) {
        va[i] = __builtin_amdgcn_raw_buffer_load_b128(rv, vb, vs ^ ((i & 7) * 512 + (i >> 3) * 256), 2);
        __builtin_amdgcn_sched_barrier(0);
      }
    }
    __builtin_amdgcn_s_setprio(0);
    lwait(FL + F_Q, 1);
    v4u qf[4];
#pragma unroll
    for (int i = 0; i < 4; ++i) qf[i] = *(LAS v4u*)(S + L_Q + ((n >> 1) & 1) * 512 + ((i ^ (rk4 & 3)) * 128) + ((n & 1) + 2 * g) * 16);
    f4 sa[4];
#pragma unroll
    for (int t = 0; t < 4; ++t) {
      sa[t] = f4{0.f, 0.f, 0.f, 0.f};
#pragma unroll
      for (int i = 0; i < 4; ++i) sa[t] = mfma(ka[4 * t + i], qf[i], sa[t]);
    }
    const int c = n >> 2, par = n & 1;
    const f4 sc = c == 0 ? sa[0] : c == 1 ? sa[1] : c == 2 ? sa[2] : sa[3];
    const float own = par ? sc.w : sc.x, snd = par ? sc.y : sc.z;
    const float s2 = rbf(own + dpp<XOR1>(snd)) * (0.0625f * 1.4426950408889634f);
    float mx = fmaxf(s2, dpp<XOR1>(s2));
    mx = fmaxf(mx, dpp<ROR4>(mx)); mx = fmaxf(mx, dpp<ROR8>(mx)); mx = rows_max(mx);
    const float p = __builtin_amdgcn_exp2f(s2 - mx);
    float l = p + dpp<XOR1>(p);
    l += dpp<ROR4>(l); l += dpp<ROR8>(l); l = rows_sum(l);
    LAS char* pb = S + L_P + slot * 128;
    *(LAS __bf16*)(pb + ((n >> 1) & 1) * 64 + ((c ^ (rk4 >> 2)) * 8 + ((2 * g + par) ^ (rv4 & 7))) * 2) = (__bf16)p;
    if (lane == 0 || lane == 2) *(LAS f2*)(S + L_STAT + slot * 16 + lane * 4) = f2{mx, l};
    lset(FL + F_SM, w);
    STAMP(3);
    __builtin_amdgcn_sched_barrier(0);
#pragma unroll
    for (int i = 16 - VLATE; i < 16; ++i) {
      va[i] = __builtin_amdgcn_raw_buffer_load_b128(rv, vb, vs ^ ((i & 7) * 512 + (i >> 3) * 256), 2);
      __builtin_amdgcn_sched_barrier(0);
    }
    STAMP(2);
    const v4u pa = *(volatile LAS v4u*)(pb + (n >> 3) * 64 + g * 16);
    unsigned po = L_PART + slot * 2048 + (g >> 1) * 1024 + (rv4 >> 3) * 512 + (g & 1) * 256 + n * 16;
    PIN(po);
    v4u am[8];
#pragma unroll
    for (int cc = 0; cc < 8; ++cc) {
      const bool on = (n & 7) == cc;
      am[cc] = v4u{on ? pa.x : 0u, on ? pa.y : 0u, on ? pa.z : 0u, on ? pa.w : 0u};
      PIN(am[cc]);
    }
#pragma unroll
    for (int hs = 0; hs < 2; ++hs) {
      v4u f[8];
#pragma unroll
      for (int jj = 0; jj < 4; ++jj) {
#pragma unroll
        for (int cc = 0; cc < 8; ++cc) f[cc][jj] = __builtin_amdgcn_perm(va[8 * hs + 2 * jj + 1][cc >> 1], va[8 * hs + 2 * jj][cc >> 1], (cc & 1) ? 0x07060302u : 0x05040100u);
        if (jj < 3) __builtin_amdgcn_sched_barrier(0);
      }
      f4 o = {0.f, 0.f, 0.f, 0.f};
#pragma unroll
      for (int cc = 0; cc < 8; ++cc) o = mfma(am[cc], f[cc], o);
      *(LAS f4*)(S + (po ^ (hs * 512))) = o;
      __builtin_amdgcn_sched_barrier(0);
    }
    lset(FL + F_PV, w);
    __builtin_amdgcn_s_setprio(3);
    STAMP(4);
  } else {
    if (w == 0) {
      v4u q[2];
      poll<2, 1024, false>(q, ws, WS_Q + (b * QS + x * 128 + lane) * 16, tag);
#pragma unroll
      for (int h = 0; h < 2; ++h)
        *(LAS v2u*)(S + L_Q + h * 512 + lane * 8) = v2u{__builtin_amdgcn_perm(q[h].y, q[h].x, 0x07060302u), __builtin_amdgcn_perm(q[h].w, q[h].z, 0x07060302u)};
      lset(FL + F_Q, 0);
      STAMP(2);
    }
    if (w < 2) {
      const int h = w;
      lwait(FL + F_SM, nun);
      const bool live = n < nun;
      const f2 ml = *(volatile LAS f2*)(S + L_STAT + (live ? n : 0) * 16 + h * 8);
      float M = fmaxf(ml.x, dpp<XOR1>(ml.x));
      M = fmaxf(M, dpp<XOR2>(M)); M = fmaxf(M, dpp<ROR4>(M)); M = fmaxf(M, dpp<ROR8>(M));
      const float be = live ? __builtin_amdgcn_exp2f(ml.x - M) : 0.f;
      float Ls = be * ml.y;
      Ls += dpp<XOR1>(Ls); Ls += dpp<XOR2>(Ls); Ls += dpp<ROR4>(Ls); Ls += dpp<ROR8>(Ls);
      const float wt = be * __builtin_amdgcn_rcpf(Ls);
      const unsigned lse = asu(M + __builtin_amdgcn_logf(Ls));
      unsigned g1 = __builtin_amdgcn_perm(lse, tag, 0x07060100u), g3 = __builtin_amdgcn_perm(lse, tag, 0x05040302u);
      float wk[10];
#pragma unroll
      for (int i = 0; i < 10; ++i) wk[i] = asf(__builtin_amdgcn_readlane(asu(wt), i));
      int aoff = WS_ATT + ((((x * 8 + b) * 2 + h) * 4 + j) * 64 + lane) * 16;
      unsigned pa0 = L_PART + h * 1024 + lane * 16;
      PIN(g1); PIN(g3); PIN(aoff); PIN(pa0);
      f4 a[10];
#pragma nounroll
      for (int it = 0; it < SPIN_L; ++it) {
        const unsigned done = FL[F_PV + n];
#pragma unroll
        for (int i = 0; i < 10; ++i) a[i] = *(volatile LAS f4*)(S + pa0 + (i < 4 || i < nun ? i * 2048 : 0));
        if (lseen(done) >= nun) break;
      }
      const f4 acc = ((a[0] * wk[0] + a[1] * wk[1]) + (a[2] * wk[2] + a[3] * wk[3])) + (((a[4] * wk[4] + a[5] * wk[5]) + (a[6] * wk[6] + a[7] * wk[7])) + (a[8] * wk[8] + a[9] * wk[9]));
      put(ws, aoff, v4u{pk(acc.x, acc.y), g1, pk(acc.z, acc.w), g3});
      lset(FL + F_PUB, w);
      STAMP(4);
    }
  }

  if (isS1) {
    if (w >= 4) {
      const int bb = w - 4;
      unsigned so = L_ACT + bb * AS + (lane >> 5) * 256 + n * 16 + (g & 1) * 8;
      PIN(so);
      lwait(FL + F_PV, nun);
      v4u pq[8];
      poll_att(pq, ws, WS_ATT + ((x * 8 + bb) * 512 + lane) * 16, WS_ATT + ((x * 8 + bb) * 512 + (lane & 7) * 64 + 63) * 16, tag);
#pragma unroll
      for (int h = 0; h < 2; ++h) {
        const v4u* pr = pq + 4 * h;
        const float l0 = lseA(pr[0]), l1 = lseA(pr[1]), l2 = lseA(pr[2]), l3 = lseA(pr[3]);
        const float M = fmaxf(fmaxf(l0, l1), fmaxf(l2, l3));
        const float e0 = __builtin_amdgcn_exp2f(l0 - M), e1 = __builtin_amdgcn_exp2f(l1 - M), e2 = __builtin_amdgcn_exp2f(l2 - M), e3 = __builtin_amdgcn_exp2f(l3 - M);
        const float inv = __builtin_amdgcn_rcpf((e0 + e1) + (e2 + e3));
        const float o0 = (e0 * blo(pr[0].x) + e1 * blo(pr[1].x)) + (e2 * blo(pr[2].x) + e3 * blo(pr[3].x));
        const float o1 = (e0 * bhi(pr[0].x) + e1 * bhi(pr[1].x)) + (e2 * bhi(pr[2].x) + e3 * bhi(pr[3].x));
        const float o2 = (e0 * blo(pr[0].z) + e1 * blo(pr[1].z)) + (e2 * blo(pr[2].z) + e3 * blo(pr[3].z));
        const float o3 = (e0 * bhi(pr[0].z) + e1 * bhi(pr[1].z)) + (e2 * bhi(pr[2].z) + e3 * bhi(pr[3].z));
        *(LAS v2u*)(S + so + h * 512) = v2u{pk(o0 * inv, o1 * inv), pk(o2 * inv, o3 * inv)};
      }
      STAMP(5);
      BARRIER();
    } else {
      v4u wf[16];
      unsigned tb = L_TILB + tr_base(lane, w);
      PIN(tb);
      lwait(FL + F_TILB, 2);
#pragma unroll
      for (int ks = 0; ks < 16; ++ks) wf[ks] = trfrag(S + tb + ks * 4096);
      unsigned ao = L_ACT + (n & 7) * AS + g * 16;
      int off = n < 8 ? WS_S1P + (n * S1S + (b * 8 + x) * 16 + w * 4 + g) * 16 : 0x7ffffff0;
      PIN(ao); PIN(off);
      STAMP(5);
      BARRIER();
      v4u bq[16];
#pragma unroll
      for (int ks = 0; ks < 16; ++ks) bq[ks] = *(LAS v4u*)(S + ao + ks * 64);
      __builtin_amdgcn_sched_barrier(0);
      f4 a0 = {0.f, 0.f, 0.f, 0.f};
#pragma unroll
      for (int ks = 0; ks < 16; ++ks) a0 = mfma(wf[ks], bq[ks], a0);
      put(ws, off, enc4(a0, tag));
      STAMP(6);
    }
  } else if (isF) {
    if (w < 4) {
      v4u f1[14], f3[14];
      lwait(FL + F_PUB, 2);
      dma_tile<true>(mk(w3, D * 3584), fidx * 128, 3584, w * 2, 8, 7, lb + L_TILB + w * 2048, 8192, lane);
      asm volatile("s_waitcnt vmcnt(0)" ::: "memory");
      lset(FL + F_TILE, w);
      STAMP(1);
      lwait(FL + F_TILE, 4);
      unsigned tb = L_TILE + tr_base(lane, w), tb3 = tb + L_TILB;
      PIN(tb); PIN(tb3);
#pragma unroll
      for (int ks = 0; ks < 14; ++ks) { f1[ks] = trfrag(S + tb + ks * 4096); f3[ks] = trfrag(S + tb3 + ks * 4096); }
      unsigned ao = L_ACT + (n & 7) * AS + g * 16, ho = L_HH + (n & 7) * HS + w * 32 + g * 8;
      PIN(ao); PIN(ho);
      STAMP(5);
      BARRIER();
      v4u bq[14];
#pragma unroll
      for (int ks = 0; ks < 7; ++ks) bq[ks] = *(LAS v4u*)(S + ao + ks * 64);
      __builtin_amdgcn_sched_barrier(0);
      f4 ga = {0.f, 0.f, 0.f, 0.f}, ua = ga;
#pragma unroll
      for (int ks = 0; ks < 14; ++ks) {
        ga = mfma(f1[ks], bq[ks], ga); ua = mfma(f3[ks], bq[ks], ua);
        if (ks < 7) bq[ks + 7] = *(LAS v4u*)(S + ao + (ks + 7) * 64);
        __builtin_amdgcn_sched_barrier(0);
      }
      float hh[4];
#pragma unroll
      for (int i = 0; i < 4; ++i) hh[i] = rbf(silu(rbf(ga[i]))) * rbf(ua[i]);
      *(LAS v2u*)(S + ho) = v2u{pk(hh[0], hh[1]), pk(hh[2], hh[3])};
      STAMP(6);
      BARRIER();
    } else {
      const int bb = w - 4;
      lwait(FL + F_PUB, 2);
      v2u wr[16];
      const rsrc_t r2 = mk(w2, 1792 * 896);
      const int wb = (fidx * 64 + 8 * g) * 896 + (bb < 7 ? bb : 6) * 128 + (4 * (n & 3) + (n >> 2)) * 8;
#pragma unroll
      for (int i = 0; i < 16; ++i) wr[i] = __builtin_amdgcn_raw_buffer_load_b64(r2, wb + ((i >> 3) * 32 + (i & 7)) * 896, 0, 2);
      v4u m[4];
      poll<4, 896, false>(m, ws, WS_MOD + (bb * MODS + 336 + L) * 16, tag);
      v4u f2_[8];
#pragma unroll
      for (int t = 0; t < 2; ++t)
#pragma unroll
        for (int cc = 0; cc < 4; ++cc)
#pragma unroll
          for (int jj = 0; jj < 4; ++jj) f2_[t * 4 + cc][jj] = __builtin_amdgcn_perm(wr[8 * t + 2 * jj + 1][cc >> 1], wr[8 * t + 2 * jj][cc >> 1], (cc & 1) ? 0x07060302u : 0x05040100u);
#pragma unroll
      for (int i = 0; i < 8; ++i) PIN(f2_[i]);
      f4 sh[2], sc[2];
#pragma unroll
      for (int i = 0; i < 8; ++i) { sh[i >> 2][i & 3] = bhi(m[i >> 2][i & 3]); sc[i >> 2][i & 3] = rbf(1.0f + bhi(m[2 + (i >> 2)][i & 3])); }
      unsigned ao = L_ACT + bb * AS + L * 8, ho = L_HH + (n & 7) * HS + g * 16;
      float cnt = act ? 1.0f / D : 0.f;
      int off = WS_S3P + ((n & 7) * S3S + (bb * 28 + fidx) * 16 + (n >> 3) * 4 + g) * 16;
      PIN(sh[0]); PIN(sh[1]); PIN(sc[0]); PIN(sc[1]); PIN(ao); PIN(ho); PIN(off); PIN(cnt);
      lwait(FL + F_TILE, 4);
      STAMP(5);
      v4u xg[2];
      poll<2, 896, false>(xg, ws, WS_X1 + (bb * 112 + L) * 16, tag);
      STAMP(6);
      f4 v0 = {bhi(xg[0].x), bhi(xg[0].y), bhi(xg[0].z), bhi(xg[0].w)}, v1 = {bhi(xg[1].x), bhi(xg[1].y), bhi(xg[1].z), bhi(xg[1].w)};
      f4 t = v0 + v1;
      const float mean = wave_sum(((t.x + t.y) + (t.z + t.w)) * cnt);
      const f4 d0 = v0 - mean, d1 = v1 - mean;
      t = d0 * d0 + d1 * d1;
      const float var = rbf(wave_sum(((t.x + t.y) + (t.z + t.w)) * cnt));
      const float mb = rbf(mean);
      v0 = (v0 - mb) * sc[0]; v1 = (v1 - mb) * sc[1];
      const float inv = __builtin_amdgcn_rcpf(rbf(__builtin_amdgcn_sqrtf(rbf(var + 1e-5f))));
      v0 = v0 * inv + sh[0]; v1 = v1 * inv + sh[1];
      *(LAS v2u*)(S + ao) = v2u{pk(v0.x, v0.y), pk(v0.z, v0.w)};
      *(LAS v2u*)(S + ao + 448) = v2u{pk(v1.x, v1.y), pk(v1.z, v1.w)};
      asm volatile("s_waitcnt lgkmcnt(0)\n\ts_barrier\n\ts_barrier" ::: "memory");
      if (bb < 7) {
        const v4u h0 = *(LAS v4u*)(S + ho), h1 = *(LAS v4u*)(S + ho + 64);
        f4 d[4];
#pragma unroll
        for (int cc = 0; cc < 4; ++cc) d[cc] = mfma(f2_[4 + cc], h1, mfma(f2_[cc], h0, f4{0.f, 0.f, 0.f, 0.f}));
        const bool hi = n >> 3;
        put(ws, off, enc4(f4{hi ? d[0].y : d[0].x, hi ? d[1].y : d[1].x, hi ? d[2].y : d[2].x, hi ? d[3].y : d[3].x}, tag));
        put(ws, off + 128, enc4(f4{hi ? d[0].w : d[0].z, hi ? d[1].w : d[1].z, hi ? d[2].w : d[2].z, hi ? d[3].w : d[3].z}, tag));
      }
      STAMP(7);
    }
  } else if (j == 2 && x < 7 && w == 2) {
      const v2u xv = __builtin_amdgcn_raw_buffer_load_b64(mk(xin, 8 * D * 2), b * 896 + x * 128 + n * 8, 0, 0);
      lwait(FL + F_PUB, 2);
      v4u gg[2];
      poll<2, 336 * 16, false>(gg, ws, WS_MOD + (b * MODS + 224 + x * 16 + n) * 16, tag);
      f4 xf = {blo(xv.x), bhi(xv.x), blo(xv.y), bhi(xv.y)}, gaf = {bhi(gg[0].x), bhi(gg[0].y), bhi(gg[0].z), bhi(gg[0].w)}, gbf = {bhi(gg[1].x), bhi(gg[1].y), bhi(gg[1].z), bhi(gg[1].w)};
      int o1 = lane < 16 ? WS_X1 + (b * 112 + x * 16 + n) * 16 : 0x7ffffff0, o3 = WS_S3P + (b * S3S + x * 448 + lane) * 16, oo = lane < 16 ? b * 896 + x * 128 + n * 8 : 0x7ffffff0;
      PIN(xf); PIN(gaf); PIN(gbf); PIN(o1); PIN(o3); PIN(oo);
      v4u p1[2];
      poll<2, 1024, false>(p1, ws, WS_S1P + (b * S1S + x * 128 + lane) * 16, tag);
      STAMP(1);
      f4 a = dec4(p1[0]) + dec4(p1[1]);
      f4 x1;
#pragma unroll
      for (int i = 0; i < 4; ++i) x1[i] = rbf(xf[i] + rbf(gaf[i] * rbf(rows_sum(a[i]))));
      put(ws, o1, enc4b(x1, tag));
      STAMP(5);
      v4u p3[7];
      poll<7, 1024, false>(p3, ws, o3, tag);
      STAMP(6);
      a = ((dec4(p3[0]) + dec4(p3[1])) + (dec4(p3[2]) + dec4(p3[3]))) + ((dec4(p3[4]) + dec4(p3[5])) + dec4(p3[6]));
      f4 y;
#pragma unroll
      for (int i = 0; i < 4; ++i) y[i] = x1[i] + rbf(gbf[i] * rbf(rows_sum(a[i])));
      __builtin_amdgcn_raw_buffer_store_b64(v2u{pk(y[0], y[1]), pk(y[2], y[3])}, mk(out, 8 * D * 2), oo, 0, 0);
      if (idx == 0 && lane == 0) __builtin_amdgcn_raw_buffer_store_b32(tag, ws, WS_EPOCH, 0, 16);
      STAMP(7);
  }
}

#undef kc
#undef vc
#undef ws_
#undef wmod
#undef wq
#undef cin
#undef xin
#undef wo
#undef w1
#undef w3
#undef w2
#undef out
#undef tag

extern "C" void kernel_launch(void* const* d_in, const int* in_sizes, int n_in, void* d_out, int out_size, void* d_ws, size_t ws_size,
                              hipStream_t stream) {
  fused_decode<<<256, 768, 0, stream>>>((const char*)d_in[1], (const char*)d_in[2], (char*)d_ws, (const char*)d_in[10], (const char*)d_in[3],
                                        (const char*)d_in[9], (const char*)d_in[0], (const char*)d_in[4], (const char*)d_in[6],
                                        (const char*)d_in[7], (const char*)d_in[8], (char*)d_out);
}
```

```cpp
#include <hip/hip_runtime.h>
#include <stdint.h>

#define LAS __attribute__((address_space(3)))
typedef unsigned v4u __attribute__((ext_vector_type(4)));
typedef unsigned v2u __attribute__((ext_vector_type(2)));
typedef float f4 __attribute__((ext_vector_type(4)));
typedef float f2 __attribute__((ext_vector_type(2)));
typedef __bf16 bfx8 __attribute__((ext_vector_type(8)));
typedef __bf16 bf2 __attribute__((ext_vector_type(2)));
typedef short s4 __attribute__((ext_vector_type(4)));
typedef unsigned long long u64;
typedef __amdgpu_buffer_rsrc_t rsrc_t;

namespace {
constexpr int D = 448;
constexpr int WS_EPOCH = 0;
constexpr int MODS = 688, QS = 1040, S1S = 7 * 16 * 16 + 16, S3S = 28 * 16 + 16;
constexpr int WS_MOD = 4096;
constexpr int WS_Q = WS_MOD + 8 * MODS * 16;
constexpr int WS_ATT = WS_Q + 8 * QS * 16;
constexpr int WS_S1P = WS_ATT + 16 * 8 * 4 * 64 * 16;
constexpr int WS_X1 = WS_S1P + 8 * S1S * 16;
constexpr int WS_S3P = WS_X1 + 8 * 112 * 16;
constexpr int WS_END = WS_S3P + 56 * S3S * 16;
constexpr int L_TILE = 0;
constexpr int L_TILB = 57344;
constexpr int L_PART = L_TILB + 57344;
constexpr int L_ACT = L_PART + 9 * 2048;
constexpr int AS = 1056;
constexpr int L_HH = L_ACT + 8 * AS;
constexpr int HS = 160;
constexpr int L_Q = L_HH + 8 * HS;
constexpr int L_P = L_Q + 1024;
constexpr int L_STAT = L_P + 10 * 128;
constexpr int L_FLAG = L_STAT + 10 * 16;
constexpr int L_DEAD = L_FLAG + 512;
constexpr int L_END = L_DEAD + 2048;
constexpr int F_SM = 0, F_PV = 16, F_Q = 32, F_TILB = 48, F_TILE = 64, F_ACT = 80, F_GO = 96, F_PUB = 112;
constexpr int DLY_P = 5, DLY_KV = 18, ASLP = 4, RSLP = 60, PFN = 6, PQN = 16;
constexpr int CODE_KB = 19;
constexpr int SPIN_G = 1 << 20, SPIN_L = 1 << 22;

__device__ __forceinline__ rsrc_t mk(const void* p, int n) { return __builtin_amdgcn_make_buffer_rsrc((void*)p, 0, n, 0x00020000); }
__device__ __forceinline__ float asf(unsigned u) { return __builtin_bit_cast(float, u); }
__device__ __forceinline__ unsigned asu(float f) { return __builtin_bit_cast(unsigned, f); }
__device__ __forceinline__ unsigned pk(float lo, float hi) { bf2 v = {(__bf16)lo, (__bf16)hi}; return __builtin_bit_cast(unsigned, v); }
__device__ __forceinline__ float rbf(float f) { return (float)(__bf16)f; }
__device__ __forceinline__ float blo(unsigned u) { return asf(u << 16); }
__device__ __forceinline__ float bhi(unsigned u) { return asf(u & 0xffff0000u); }
__device__ __forceinline__ f4 mfma(v4u a, v4u b, f4 c) {
  return __builtin_amdgcn_mfma_f32_16x16x32_bf16(__builtin_bit_cast(bfx8, a), __builtin_bit_cast(bfx8, b), c, 0, 0, 0);
}
typedef unsigned v8u __attribute__((ext_vector_type(8)));
typedef __bf16 bf16x __attribute__((ext_vector_type(16)));
__device__ __forceinline__ f4 mfma2(v4u a, v8u b, f4 c, int sel) {
  return __builtin_amdgcn_smfmac_f32_16x16x64_bf16(__builtin_bit_cast(bfx8, a), __builtin_bit_cast(bf16x, b), c, sel, 0, 0);
}
__device__ __forceinline__ float halves(float a, float b) {
  auto s = __builtin_amdgcn_permlane32_swap(asu(a), asu(b), false, false);
  return asf(s[0]) + asf(s[1]);
}
__device__ __forceinline__ f4 halves4(f4 a, f4 b) {
#pragma unroll
  for (int i = 0; i < 4; ++i) { auto s = __builtin_amdgcn_permlane32_swap(asu(a[i]), asu(b[i]), false, false); a[i] = asf(s[0]); b[i] = asf(s[1]); }
  return a + b;
}
template <int CTRL> __device__ __forceinline__ float dpp(float x) { return asf(__builtin_amdgcn_mov_dpp(asu(x), CTRL, 0xf, 0xf, true)); }
constexpr int XOR1 = 0xB1, XOR2 = 0x4E, ROR4 = 0x124, ROR8 = 0x128;
__device__ __forceinline__ float rows_sum(float x) {
  auto s = __builtin_amdgcn_permlane16_swap(asu(x), asu(x), false, false);
  x = asf(s[0]) + asf(s[1]);
  auto t = __builtin_amdgcn_permlane32_swap(asu(x), asu(x), false, false);
  return asf(t[0]) + asf(t[1]);
}
__device__ __forceinline__ float scatter_sum(f4 v) {
  auto s = __builtin_amdgcn_permlane32_swap(asu(v.x), asu(v.z), false, false), t = __builtin_amdgcn_permlane32_swap(asu(v.y), asu(v.w), false, false);
  auto u = __builtin_amdgcn_permlane16_swap(asu(asf(s[0]) + asf(s[1])), asu(asf(t[0]) + asf(t[1])), false, false);
  return asf(u[0]) + asf(u[1]);
}
__device__ __forceinline__ float rows_max(float x) {
  auto s = __builtin_amdgcn_permlane16_swap(asu(x), asu(x), false, false);
  x = fmaxf(asf(s[0]), asf(s[1]));
  auto t = __builtin_amdgcn_permlane32_swap(asu(x), asu(x), false, false);
  return fmaxf(asf(t[0]), asf(t[1]));
}
__device__ __forceinline__ float wave_sum(float x) {
  x += dpp<XOR1>(x); x += dpp<XOR2>(x); x += dpp<ROR4>(x); x += dpp<ROR8>(x);
  return rows_sum(x);
}
#define CBAR() asm volatile("" ::: "memory")

__device__ __forceinline__ v4u enc4(f4 v, unsigned tag) {
  return v4u{__builtin_amdgcn_perm(tag, asu(v.x), 0x03020104u), __builtin_amdgcn_perm(tag, asu(v.y), 0x03020105u),
             __builtin_amdgcn_perm(tag, asu(v.z), 0x03020106u), __builtin_amdgcn_perm(tag, asu(v.w), 0x03020107u)};
}
__device__ __forceinline__ unsigned tag4(v4u g) {
  return __builtin_amdgcn_perm(g.y, g.x, 0x0c0c0400u) | __builtin_amdgcn_perm(g.w, g.z, 0x04000c0cu);
}
__device__ __forceinline__ f4 dec4(v4u g) { return f4{asf(g.x & ~255u), asf(g.y & ~255u), asf(g.z & ~255u), asf(g.w & ~255u)}; }
__device__ __forceinline__ void put(rsrc_t ws, int off, v4u g) { __builtin_amdgcn_raw_buffer_store_b128(g, ws, off, 0, 16); }
__device__ __forceinline__ float lseA(v4u g) { return asf(__builtin_amdgcn_perm(g.y, g.w, 0x07060302u)); }

template <int N, int STEP, int SLP = 0>
__device__ __forceinline__ void poll(v4u (&x)[N], rsrc_t ws, int off, unsigned tag) {
  unsigned left = SPIN_G;
  u64 keep, miss;
#pragma nounroll
  do {
    if (SLP) __builtin_amdgcn_s_sleep(SLP);
    asm volatile("s_sub_u32 %0, %0, 1\n\ts_cselect_b64 %1, 0, -1" : "+s"(left), "=s"(keep) :: "scc", "memory");
#pragma unroll
    for (int l = 0; l < N; ++l) x[l] = __builtin_amdgcn_raw_buffer_load_b128(ws, off + l * STEP, 0, 16);
    unsigned bad = tag4(x[0]) ^ tag;
#pragma unroll
    for (int l = 1; l < N; ++l) { asm("" : "+v"(bad)); bad |= tag4(x[l]) ^ tag; }
    asm("" : "+v"(bad));
    miss = __builtin_amdgcn_ballot_w64(bad != 0) & keep;
  } while (miss != 0);
}
__device__ __forceinline__ void poll_att(v4u (&p)[4], rsrc_t ws, int off, int poff, unsigned tg) {
  unsigned need = 15u, left = SPIN_G, tr = 0u, rest, t0, t1;
  asm volatile(
      "0:\n\t"
      "s_cmp_lg_u32 %[tr], 0\n\ts_cbranch_scc1 1f\n\t"
      "s_bcnt1_i32_b32 vcc_lo, %[need]\n\ts_cmp_le_u32 vcc_lo, 1\n\ts_cbranch_scc0 6f\n\t"
      "s_mov_b32 %[tr], %[need]\n"
      "1:\n\t"
      "s_bitcmp1_b32 %[tr], 0\n\ts_cbranch_scc0 10f\n\tbuffer_load_dwordx4 v[64:67], %[off], %[rs], 0 offen sc1\n"
      "10:\n\t"
      "s_bitcmp1_b32 %[tr], 1\n\ts_cbranch_scc0 11f\n\tbuffer_load_dwordx4 v[68:71], %[off], %[rs], 0 offen offset:1024 sc1\n"
      "11:\n\t"
      "s_bitcmp1_b32 %[tr], 2\n\ts_cbranch_scc0 12f\n\tbuffer_load_dwordx4 v[72:75], %[off], %[rs], 0 offen offset:2048 sc1\n"
      "12:\n\t"
      "s_bitcmp1_b32 %[tr], 3\n\ts_cbranch_scc0 6f\n\tbuffer_load_dwordx4 v[76:79], %[off], %[rs], 0 offen offset:3072 sc1\n"
      "6:\n\t"
      "s_andn2_b32 %[rest], %[need], %[tr]\n\ts_cbranch_scc0 7f\n\t"
      "buffer_load_dwordx4 v[80:83], %[poff], %[rs], 0 offen sc1\n\ts_waitcnt vmcnt(0)\n\t"
      "v_perm_b32 %[t0], v83, v81, %[sel]\n\tv_cmp_eq_u32 vcc, %[t0], %[tg]\n\ts_nop 0\n\t"
      "s_and_b32 %[rest], %[rest], vcc_lo\n\ts_branch 8f\n"
      "7:\n\t"
      "s_waitcnt vmcnt(0)\n\t"
      "v_perm_b32 %[t0], v67, v65, %[sel]\n\tv_perm_b32 %[t1], v71, v69, %[sel]\n\tv_perm_b32 v80, v75, v73, %[sel]\n\tv_perm_b32 v81, v79, v77, %[sel]\n\t"
      "v_bitop3_b32 %[t0], %[t0], %[t1], %[tg] bitop3:0x7e\n\tv_bitop3_b32 %[t1], v80, v81, %[tg] bitop3:0x7e\n\t"
      "v_or_b32 %[t0], %[t0], %[t1]\n\tv_cmp_ne_u32 vcc, 0, %[t0]\n\ts_cbranch_vccz 5f\n"
      "8:\n\t"
      "s_bitcmp1_b32 %[tr], 0\n\ts_cbranch_scc0 20f\n\tv_perm_b32 %[t0], v67, v65, %[sel]\n\tv_cmp_ne_u32 vcc, %[tg], %[t0]\n\t"
      "s_cbranch_vccnz 20f\n\ts_bitset0_b32 %[need], 0\n"
      "20:\n\t"
      "s_bitcmp1_b32 %[tr], 1\n\ts_cbranch_scc0 21f\n\tv_perm_b32 %[t0], v71, v69, %[sel]\n\tv_cmp_ne_u32 vcc, %[tg], %[t0]\n\t"
      "s_cbranch_vccnz 21f\n\ts_bitset0_b32 %[need], 1\n"
      "21:\n\t"
      "s_bitcmp1_b32 %[tr], 2\n\ts_cbranch_scc0 22f\n\tv_perm_b32 %[t0], v75, v73, %[sel]\n\tv_cmp_ne_u32 vcc, %[tg], %[t0]\n\t"
      "s_cbranch_vccnz 22f\n\ts_bitset0_b32 %[need], 2\n"
      "22:\n\t"
      "s_bitcmp1_b32 %[tr], 3\n\ts_cbranch_scc0 23f\n\tv_perm_b32 %[t0], v79, v77, %[sel]\n\tv_cmp_ne_u32 vcc, %[tg], %[t0]\n\t"
      "s_cbranch_vccnz 23f\n\ts_bitset0_b32 %[need], 3\n"
      "23:\n\t"
      "s_and_b32 %[tr], %[tr], %[need]\n\ts_or_b32 %[tr], %[tr], %[rest]\n\t"
      "s_cmp_lg_u32 %[rest], 0\n\ts_cbranch_scc1 9f\n\ts_sleep %c[slp]\n"
      "9:\n\t"
      "s_sub_u32 %[left], %[left], 1\n\ts_cbranch_scc0 0b\n"
      "5:"
      : "={v[64:67]}"(p[0]), "={v[68:71]}"(p[1]), "={v[72:75]}"(p[2]), "={v[76:79]}"(p[3]), [need] "+s"(need), [left] "+s"(left),
        [tr] "+s"(tr), [rest] "=&s"(rest), [t0] "=&v"(t0), [t1] "=&v"(t1), [off] "+v"(off), [poff] "+v"(poff), [tg] "+v"(tg)
      : [rs] "s"(ws), [sel] "s"(0x05040100u), [slp] "i"(ASLP)
      : "vcc", "scc", "memory", "v80", "v81", "v82", "v83");
}
__device__ __forceinline__ int lseen(unsigned v) { return __builtin_popcount((unsigned)__builtin_amdgcn_ballot_w64(v != 0u) & 0xffffu); }
template <int M = 15>
__device__ __forceinline__ void lwait(volatile LAS unsigned* f, int want) {
  volatile LAS unsigned* p = f + (threadIdx.x & M);
#pragma nounroll
  for (int it = 0; it < SPIN_L; ++it) {
    if (lseen(*p) >= want) break;
    __builtin_amdgcn_s_sleep(1);
  }
  CBAR();
}
__device__ __forceinline__ void lwait4(volatile LAS unsigned* f) {
  volatile LAS unsigned* p = f + (threadIdx.x & 3);
#pragma nounroll
  for (int it = 0; it < SPIN_L; ++it) {
    if (__builtin_amdgcn_ballot_w64(*p == 0u) == 0) break;
    __builtin_amdgcn_s_sleep(1);
  }
  CBAR();
}
__device__ __forceinline__ void lset(volatile LAS unsigned* f, int w) { CBAR(); f[w] = 1u; }
template <bool NT>
__device__ __forceinline__ void dma(rsrc_t rs, int voff, int soff, unsigned ldsaddr) {
  unsigned keep;
  if (NT)
    asm volatile("s_mov_b32 %0, m0\n\ts_mov_b32 m0, %3\n\ts_nop 0\n\tbuffer_load_dwordx4 %1, %2, %4 offen nt lds\n\ts_mov_b32 m0, %0"
                 : "=&s"(keep) : "v"(voff), "s"(rs), "s"(ldsaddr), "s"(soff) : "memory");
  else
    asm volatile("s_mov_b32 %0, m0\n\ts_mov_b32 m0, %3\n\ts_nop 0\n\tbuffer_load_dwordx4 %1, %2, %4 offen lds\n\ts_mov_b32 m0, %0"
                 : "=&s"(keep) : "v"(voff), "s"(rs), "s"(ldsaddr), "s"(soff) : "memory");
}
template <bool NT>
__device__ __forceinline__ void dma_tile(rsrc_t rs, int base, int stride, int q0, int dq, int n, unsigned lds, int dl, int lane) {
  const int rowoff = (lane >> 3) * stride;
  const int v0 = rowoff + (((lane & 7) ^ (((lane >> 4) & 1) << 1)) << 4), v1 = v0 ^ 64;
  int so = base + q0 * 8 * stride;
  unsigned la = lds;
#pragma nounroll
  for (int i = 0; i < n; ++i) {
    dma<NT>(rs, v0, so, la);
    dma<NT>(rs, v1, so + 8 * stride, la + 1024);
    so += dq * 8 * stride; la += dl;
  }
}
__device__ __forceinline__ v4u trfrag(LAS char* p) {
  s4 a = __builtin_amdgcn_ds_read_tr16_b64_v4i16((LAS s4*)p), b = __builtin_amdgcn_ds_read_tr16_b64_v4i16((LAS s4*)(p + 512));
  v2u x = __builtin_bit_cast(v2u, a), y = __builtin_bit_cast(v2u, b);
  return v4u{x.x, x.y, y.x, y.y};
}
__device__ __forceinline__ v8u trfrag2(LAS char* p) {
  const v4u a = trfrag(p), b = trfrag(p + 4096);
  return v8u{a.x, a.y, a.z, a.w, b.x, b.y, b.z, b.w};
}
__device__ __forceinline__ int tr_base(int lane, int nt) {
  const int g = lane >> 4, q4 = (lane & 15) >> 2, p = lane & 3;
  return (8 * g + q4) * 128 + ((nt ^ ((q4 >> 1) | ((g & 1) << 1))) << 5) + p * 8;
}
__device__ __forceinline__ void ln_norm(float (&x)[8], bool act) {
  float s = 0.f;
#pragma unroll
  for (int i = 0; i < 8; ++i) s += x[i];
  const float mean = wave_sum(act ? s : 0.f) * (1.0f / D);
  float q = 0.f;
#pragma unroll
  for (int i = 0; i < 8; ++i) { const float d = x[i] - mean; q += d * d; }
  const float var = rbf(wave_sum(act ? q : 0.f) * (1.0f / D));
  const float mb = rbf(mean);
  const float inv = __builtin_amdgcn_rcpf(rbf(__builtin_amdgcn_sqrtf(rbf(var + 1e-5f))));
#pragma unroll
  for (int i = 0; i < 8; ++i) x[i] = rbf(rbf(x[i] - mb) * inv);
}
__device__ __forceinline__ void mod_store(const float (&ln)[8], const v4u (&m)[4], LAS char* row, int L) {
  float h[8];
#pragma unroll
  for (int i = 0; i < 8; ++i) {
    const float sh = bhi(m[i >> 2][i & 3]), sc = bhi(m[2 + (i >> 2)][i & 3]);
    h[i] = rbf(ln[i] * rbf(1.0f + sc)) + sh;
  }
  *(LAS v2u*)(row + L * 8) = v2u{pk(h[0], h[1]), pk(h[2], h[3])};
  *(LAS v2u*)(row + 448 + L * 8) = v2u{pk(h[4], h[5]), pk(h[6], h[7])};
}
__device__ __forceinline__ float silu(float v) {
  const float d = 1.0f + __builtin_amdgcn_exp2f(fminf(-1.4426950408889634f * v, 126.0f)), r = __builtin_amdgcn_rcpf(d), q = v * r;
  return __builtin_fmaf(__builtin_fmaf(-q, d, v), r, q);
}
#define PIN(x) asm volatile("" : "+v"(x))
__device__ __forceinline__ void touch(rsrc_t r, int off) { unsigned v = __builtin_amdgcn_raw_buffer_load_b32(r, off & ~3, 0, 16); asm volatile("" :: "v"(v)); }
#define BARRIER() asm volatile("s_waitcnt lgkmcnt(0)\n\ts_barrier" ::: "memory")
}

typedef unsigned s16u __attribute__((ext_vector_type(16)));
typedef unsigned s8u __attribute__((ext_vector_type(8)));
#define KPTR(A, i) ((char*)(((u64)(A)[2 * (i) + 1] << 32) | (A)[2 * (i)]))
#define kc KPTR(KA, 0)
#define vc KPTR(KA, 1)
#define ws_ KPTR(KA, 2)
#define wmod KPTR(KA, 3)
#define wq KPTR(KA, 4)
#define cin KPTR(KA, 5)
#define xin KPTR(KA, 6)
#define wo KPTR(KA, 7)
#define w1 KPTR(KB, 0)
#define w3 KPTR(KB, 1)
#define w2 KPTR(KB, 2)
#define out KPTR(KB, 3)
#define KWAIT() do { asm volatile("s_waitcnt lgkmcnt(0) ; KWAIT" : "={s[72:87]}"(KA), "={s[88:95]}"(KB)); ws = mk(ws_, WS_END); \
    int zoff = WS_EPOCH; PIN(zoff); epoch = __builtin_amdgcn_raw_buffer_load_b32(ws, zoff, 0, 16); } while (0)
#define tag (epoch + 0x01010101u)

__global__ void __launch_bounds__(768, 3)
fused_decode(const char*, const char*, char*, const char*, const char*, const char*, const char*, const char*, const char*, const char*, const char*, char*) {
  __shared__ __attribute__((aligned(16))) char smem[L_END];
  LAS char* S = (LAS char*)smem;
  volatile LAS unsigned* FL = (volatile LAS unsigned*)(S + L_FLAG);
  const int blk = blockIdx.x, lane = threadIdx.x & 63, w = __builtin_amdgcn_readfirstlane(threadIdx.x >> 6);
  const unsigned lb = (unsigned)(size_t)S;
  const int x = blk & 7, r = blk >> 3, b = r >> 2, j = r & 3;
  s16u KA; s8u KB;
  {
    u64 pc; unsigned keep;
    const v2u zero = {0u, 0u};
    asm volatile(
        "s_cmp_lg_u32 %[w], 0\n\ts_cbranch_scc1 1f\n\t"
        "ds_write_b64 %[fa], %[z]\n\t"
        "s_getpc_b64 %[pc]\n\ts_mov_b32 %[k], m0\n\ts_mov_b32 m0, %[dead]\n\ts_nop 0\n\t"
        "global_load_lds_dwordx4 %[co], %[pc]\n\ts_mov_b32 m0, %[k]\n\t"
        "s_waitcnt lgkmcnt(0)\n"
        "1:\n\t"
        "s_load_dwordx16 s[72:87], %[ka], 0\n\ts_load_dwordx8 s[88:95], %[ka], 64\n\ts_barrier"
        : [pc] "=&s"(pc), [k] "=&s"(keep)
        : [w] "s"(w), [fa] "v"(lb + L_FLAG + lane * 8), [z] "v"(zero), [dead] "s"(lb + L_DEAD), [co] "v"((r < CODE_KB ? CODE_KB - 1 - r : 31 - r) * 1024 + 384 + lane * 16),
          [ka] "s"(__builtin_amdgcn_kernarg_segment_ptr())
        : "scc", "memory", "s72", "s73", "s74", "s75", "s76", "s77", "s78", "s79", "s80", "s81", "s82", "s83", "s84", "s85", "s86", "s87",
          "s88", "s89", "s90", "s91", "s92", "s93", "s94", "s95");
  }
  const int idx = b * 8 + x;
  const int jr = x * 8 + b, fidx = jr - 35;
  const bool urgent = jr < 14, fgrp = (unsigned)fidx < 28u;
  const int nun = (0x9968 >> (4 * j)) & 15, u0 = (0x170e0800 >> (8 * j)) & 255, nh = 12 - nun;
  const bool isP0 = j == 0 && jr < 35, isP1 = j == 1, isS1 = j >= 2 && b < 7, isF = j == 0 && fgrp;
  const int g = lane >> 4, n = lane & 15;
  rsrc_t ws;
  unsigned epoch;
  __builtin_amdgcn_s_setprio(3);
  const bool act = lane < 56;
  const int L = act ? lane : 55;
  const bool pq = j >= 2 && w == 2, pf = (j >= 2 && (urgent || fgrp) && w < 2) || pq;
  const int tile = isP1 || pq ? idx : fgrp ? fidx : jr;
  const bool stager = isP1 ? w < 8 : isP0 && w >= 4;
  const int row = isP1 ? w : w - 4;
  KWAIT();
  if (stager) dma<false>(mk(isP0 ? cin : xin, 8 * D * 2), L * 16, row * 896, lb + L_PART + row * 1024);
  if (!(urgent && j != 1) || pq) __builtin_amdgcn_s_sleep(DLY_P);
  if (((isP0 || isP1 || isF) && w < 4) || pf) {
    const int sa = isP1 || pq ? 8192 : fgrp ? 3584 : 5376, pn = pq ? PQN : PFN;
    const rsrc_t rt_ = mk(isP1 || pq ? wq : fgrp ? w1 : wmod, D * sa);
    dma_tile<true>(rt_, tile * 128, sa, pf ? 56 - pn * (pq ? j - 1 : 2 * j + w - 3) : w * 2, pf ? 2 : 8, pf ? pn >> 1 : 7,
                   lb + (pf ? L_DEAD : L_TILE + w * 2048), pf ? 0 : 8192, lane);
  }
  if (__builtin_expect(isP0 || stager, 0)) {
    unsigned tb = L_TILE + tr_base(lane, w), ao = L_ACT + (n & 7) * AS + g * 16;
    int off = n >= 8 ? 0x7ffff000 : (isP0 ? WS_MOD + n * MODS * 16 : WS_Q + n * QS * 16) + (tile * 16 + w * 4 + g) * 16;
    PIN(tb); PIN(ao); PIN(off);
    v4u wf[14], bq[14];
    if (!stager) {
      lwait(FL + F_ACT, 8);
#pragma unroll
      for (int ks = 0; ks < 14; ++ks) bq[ks] = *(LAS v4u*)(S + ao + ks * 64);
      asm volatile("s_waitcnt vmcnt(0)" ::: "memory");
      lset(FL + F_TILE, w);
      lwait(FL + F_TILE, 4);
#pragma unroll
      for (int ks = 0; ks < 14; ++ks) wf[ks] = trfrag(S + tb + ks * 4096);
      __builtin_amdgcn_sched_barrier(0);
      f4 acc = {0.f, 0.f, 0.f, 0.f};
#pragma unroll
      for (int ks = 0; ks < 14; ++ks) acc = mfma(wf[ks], bq[ks], acc);
      put(ws, off, enc4(f4{rbf(acc.x), rbf(acc.y), rbf(acc.z), rbf(acc.w)}, tag));
      lset(FL + F_GO, w);
    } else {
      asm volatile("s_waitcnt vmcnt(0)" ::: "memory");
      if (w < 4) {
        lset(FL + F_TILE, w);
        lwait4(FL + F_TILE);
#pragma unroll
        for (int ks = 0; ks < 14; ++ks) wf[ks] = trfrag(S + tb + ks * 4096);
      }
      const v2u a0 = *(LAS v2u*)(S + L_PART + row * 1024 + L * 8), a1 = *(LAS v2u*)(S + L_PART + row * 1024 + 448 + L * 8);
      float v[8] = {blo(a0.x), bhi(a0.x), blo(a0.y), bhi(a0.y), blo(a1.x), bhi(a1.x), blo(a1.y), bhi(a1.y)};
      LAS char* arow = S + L_ACT + row * AS;
      if (isP0) {
        *(LAS v2u*)(arow + L * 8) = v2u{pk(silu(v[0]), silu(v[1])), pk(silu(v[2]), silu(v[3]))};
        *(LAS v2u*)(arow + 448 + L * 8) = v2u{pk(silu(v[4]), silu(v[5])), pk(silu(v[6]), silu(v[7]))};
      } else {
        ln_norm(v, act);
#pragma unroll
        for (int i = 0; i < 8; ++i) PIN(v[i]);
        v4u m[4];
        lwait(FL + F_TILE, 8);
        poll<4, 896>(m, ws, WS_MOD + (row * MODS + L) * 16, tag);
        mod_store(v, m, arow, L);
      }
      lset(FL + F_ACT, row);
      if (w < 4) {
        lwait(FL + F_ACT, 8);
#pragma unroll
        for (int ks = 0; ks < 14; ++ks) bq[ks] = *(LAS v4u*)(S + ao + ks * 64);
        __builtin_amdgcn_sched_barrier(0);
        f4 acc = {0.f, 0.f, 0.f, 0.f};
#pragma unroll
        for (int ks = 0; ks < 14; ++ks) acc = mfma(wf[ks], bq[ks], acc);
        put(ws, off, enc4(f4{rbf(acc.x), rbf(acc.y), rbf(acc.z), rbf(acc.w)}, tag));
        lset(FL + F_GO, w);
      } else if (isP1 || urgent) {
        lwait(FL + F_GO, 4);
      }
    }
  }

  if (w >= nh) {
    const int slot = 11 - w, u = u0 + slot;
    const size_t kvo = ((size_t)(b * 8 + x) * 4096 + 3072 + u * 32) * 512;
    const rsrc_t rk = mk(kc + kvo, 16384), rv = mk(vc + kvo, 16384);
    const int rot = blk * 5 + (blk >> 4) + w * 3, rk4 = rot & 15, rv4 = (rot >> 1) & 15;
    v4u ka[16], va[16];
    const int vb = g * 4096 + n * 16, vs = (rv4 & 7) * 512 + (rv4 >> 3) * 256;
    {
      if (!isP0) __builtin_amdgcn_s_sleep(DLY_KV);
      __builtin_amdgcn_s_setprio(2);
      const int kb = (n >> 1) * 512 + ((n & 1) + 2 * g) * 16, ks = (rk4 >> 2) * 4096 + (rk4 & 3) * 128;
#pragma unroll
      for (int i = 0; i < 16; ++i) {
        ka[i] = __builtin_amdgcn_raw_buffer_load_b128(rk, kb, ks ^ ((i >> 2) * 4096 + (i & 3) * 128), 2);
        __builtin_amdgcn_sched_barrier(0);
      }
      __builtin_amdgcn_s_setprio(1);
      if (isP1 && w >= 8) {
        asm volatile("s_waitcnt vmcnt(0)" ::: "memory");
        lset(FL + F_TILE, w);
        lwait(FL + F_GO, 4);
      }
#pragma unroll
      for (int i = 0; i < 16; ++i) {
        va[i] = __builtin_amdgcn_raw_buffer_load_b128(rv, vb, vs ^ ((i & 7) * 512 + (i >> 3) * 256), 2);
        __builtin_amdgcn_sched_barrier(0);
      }
    }
    __builtin_amdgcn_s_setprio(0);
    lwait(FL + F_Q, 1);
    v4u qf[4];
#pragma unroll
    for (int i = 0; i < 4; ++i) qf[i] = *(LAS v4u*)(S + L_Q + ((n >> 1) & 1) * 512 + ((i ^ (rk4 & 3)) * 128) + ((n & 1) + 2 * g) * 16);
    f4 sa[4];
#pragma unroll
    for (int t = 0; t < 4; ++t) {
      sa[t] = f4{0.f, 0.f, 0.f, 0.f};
#pragma unroll
      for (int i = 0; i < 4; ++i) sa[t] = mfma(ka[4 * t + i], qf[i], sa[t]);
    }
    const int c = n >> 2, par = n & 1;
    const f4 sc = c == 0 ? sa[0] : c == 1 ? sa[1] : c == 2 ? sa[2] : sa[3];
    const float own = par ? sc.w : sc.x, snd = par ? sc.y : sc.z;
    const float s2 = rbf(own + dpp<XOR1>(snd)) * (0.0625f * 1.4426950408889634f);
    float mx = fmaxf(s2, dpp<XOR1>(s2));
    mx = fmaxf(mx, dpp<ROR4>(mx)); mx = fmaxf(mx, dpp<ROR8>(mx)); mx = rows_max(mx);
    const float p = __builtin_amdgcn_exp2f(s2 - mx);
    float l = p + dpp<XOR1>(p);
    l += dpp<ROR4>(l); l += dpp<ROR8>(l); l = rows_sum(l);
    LAS char* pb = S + L_P + slot * 128;
    *(LAS __bf16*)(pb + ((n >> 1) & 1) * 64 + ((c ^ (rk4 >> 2)) * 8 + ((2 * g + par) ^ (rv4 & 7))) * 2) = (__bf16)p;
    if (lane == 0 || lane == 2) *(LAS f2*)(S + L_STAT + slot * 16 + lane * 4) = f2{mx, l};
    lset(FL + F_SM, w);
    __builtin_amdgcn_sched_barrier(0);
    const v4u pa = *(volatile LAS v4u*)(pb + (n >> 3) * 64 + g * 16);
    unsigned po = L_PART + slot * 2048 + (g >> 1) * 1024 + (rv4 >> 3) * 512 + (g & 1) * 256 + n * 16;
    PIN(po);
    v4u am[8];
#pragma unroll
    for (int d = 0; d < 4; ++d) {
      const unsigned s0 = ((n & 7) >> 1) != d ? 0x0c0c0c0cu : n & 1 ? 0x01000c0cu : 0x0c0c0100u, s1 = ((n & 7) >> 1) != d ? 0x0c0c0c0cu : n & 1 ? 0x03020c0cu : 0x0c0c0302u;
#pragma unroll
      for (int i = 0; i < 8; ++i) am[i][d] = __builtin_amdgcn_perm(0u, pa[i >> 1], i & 1 ? s1 : s0);
    }
#pragma unroll
    for (int i = 0; i < 8; ++i) PIN(am[i]);
#pragma unroll
    for (int hs = 0; hs < 2; ++hs) {
      f4 o = {0.f, 0.f, 0.f, 0.f};
#pragma unroll
      for (int i = 0; i < 8; ++i) o = mfma(am[i], va[8 * hs + i], o);
      *(LAS f4*)(S + (po ^ (hs * 512))) = o;
      __builtin_amdgcn_sched_barrier(0);
    }
    lset(FL + F_PV, w);
    __builtin_amdgcn_s_setprio(3);
  } else {
    if (isF) { asm volatile("s_waitcnt vmcnt(0)" ::: "memory"); lset(FL + F_TILE, w); }
    if (w == 0) {
      v4u q[2];
      poll<2, 1024>(q, ws, WS_Q + (b * QS + x * 128 + lane) * 16, tag);
#pragma unroll
      for (int h = 0; h < 2; ++h)
        *(LAS v2u*)(S + L_Q + h * 512 + lane * 8) = v2u{__builtin_amdgcn_perm(q[h].y, q[h].x, 0x07060302u), __builtin_amdgcn_perm(q[h].w, q[h].z, 0x07060302u)};
      lset(FL + F_Q, 0);
    }
    if (isS1 && w == 2) {
      lwait(FL + F_SM, nun - 1);
      dma_tile<true>(mk(wo, 4096 * 896), (x * 2 + j - 2) * 256 * 896 + b * 128, 896, 0, 2, 16, lb + L_TILE, 2048, lane);
      asm volatile("s_waitcnt vmcnt(0)" ::: "memory");
      lset(FL + F_TILB, w);
    }
    if (w < 2) {
      const int h = w;
      lwait(FL + F_SM, nun);
      const bool live = n < nun;
      const f2 ml = *(volatile LAS f2*)(S + L_STAT + (live ? n : 0) * 16 + h * 8);
      float M = fmaxf(ml.x, dpp<XOR1>(ml.x));
      M = fmaxf(M, dpp<XOR2>(M)); M = fmaxf(M, dpp<ROR4>(M)); M = fmaxf(M, dpp<ROR8>(M));
      const float be = live ? __builtin_amdgcn_exp2f(ml.x - M) : 0.f;
      float Ls = be * ml.y;
      Ls += dpp<XOR1>(Ls); Ls += dpp<XOR2>(Ls); Ls += dpp<ROR4>(Ls); Ls += dpp<ROR8>(Ls);
      const float wt = be * __builtin_amdgcn_rcpf(Ls);
      const unsigned lse = asu(M + __builtin_amdgcn_logf(Ls));
      unsigned g1 = __builtin_amdgcn_perm(lse, tag, 0x07060100u), g3 = __builtin_amdgcn_perm(lse, tag, 0x05040302u);
      float wk[9];
#pragma unroll
      for (int i = 0; i < 9; ++i) wk[i] = asf(__builtin_amdgcn_readlane(asu(wt), i));
      int aoff = WS_ATT + ((((x * 8 + b) * 2 + h) * 4 + j) * 64 + lane) * 16;
      unsigned pa0 = L_PART + h * 1024 + lane * 16;
      lwait<7>(FL + F_PV, 2 * (nun - 4));
      f4 acc = *(LAS f4*)(S + pa0 + 4 * 2048) * wk[4];
#pragma unroll
      for (int i = 5; i < 9; ++i) acc += *(LAS f4*)(S + pa0 + (i < nun ? i : 4) * 2048) * wk[i];
      PIN(g1); PIN(g3); PIN(aoff); PIN(pa0); PIN(acc);
      f4 a[4];
      unsigned left = SPIN_L;
      u64 keep, miss;
#pragma nounroll
      do {
        asm volatile("s_sub_u32 %0, %0, 1\n\ts_cselect_b64 %1, 0, -1" : "+s"(left), "=s"(keep) :: "scc", "memory");
        const unsigned done = FL[F_PV + 8 + (lane & 3)];
#pragma unroll
        for (int i = 3; i >= 0; --i) a[i] = *(volatile LAS f4*)(S + pa0 + i * 2048);
        miss = __builtin_amdgcn_ballot_w64(done == 0u) & keep;
      } while (miss != 0);
#pragma unroll
      for (int i = 3; i >= 0; --i) acc += a[i] * wk[i];
      put(ws, aoff, v4u{pk(acc.x, acc.y), g1, pk(acc.z, acc.w), g3});
      lset(FL + F_PUB, w);
    }
  }

  if (isS1) {
    if (w >= 4) {
      const int bb = w - 4;
      unsigned so = L_ACT + bb * AS + (lane >> 5) * 256 + n * 16 + (g & 1) * 8;
      PIN(so);
      lwait(FL + F_PV, nun);
      lwait(FL + F_TILB, 1);
      v4u pr[4];
      const int ab = WS_ATT + ((x * 8 + bb) * 2 + j - 2) * 4096;
      poll_att(pr, ws, ab + lane * 16, ab + ((lane & 3) * 64 + 63) * 16, tag);
      const float l0 = lseA(pr[0]), l1 = lseA(pr[1]), l2 = lseA(pr[2]), l3 = lseA(pr[3]);
      float M;
      asm("v_max3_f32 %0, %1, %2, %3\n\tv_max_f32 %0, %0, %4" : "=&v"(M) : "v"(l0), "v"(l1), "v"(l2), "v"(l3));
      const float e[4] = {__builtin_amdgcn_exp2f(l0 - M), __builtin_amdgcn_exp2f(l1 - M), __builtin_amdgcn_exp2f(l2 - M), __builtin_amdgcn_exp2f(l3 - M)};
      const float inv = __builtin_amdgcn_rcpf((e[0] + e[1]) + (e[2] + e[3]));
      f4 o = f4{blo(pr[0].x), bhi(pr[0].x), blo(pr[0].z), bhi(pr[0].z)} * e[0];
#pragma unroll
      for (int i = 1; i < 4; ++i) o += f4{blo(pr[i].x), bhi(pr[i].x), blo(pr[i].z), bhi(pr[i].z)} * e[i];
      o *= inv;
      *(LAS v2u*)(S + so) = v2u{pk(o.x, o.y), pk(o.z, o.w)};
      BARRIER();
    } else {
      v4u wf[8];
      unsigned tb = L_TILE + tr_base(lane, w);
      PIN(tb);
      lwait(FL + F_TILB, 1);
#pragma unroll
      for (int ks = 0; ks < 8; ++ks) wf[ks] = trfrag(S + tb + ks * 4096);
      unsigned ao = L_ACT + (n & 7) * AS + g * 16;
      int off = n < 8 ? WS_S1P + (n * S1S + (b * 16 + x * 2 + j - 2) * 16 + w * 4 + g) * 16 : 0x7ffffff0;
      PIN(ao); PIN(off);
      BARRIER();
      v4u bq[8];
#pragma unroll
      for (int ks = 0; ks < 8; ++ks) bq[ks] = *(LAS v4u*)(S + ao + ks * 64);
      __builtin_amdgcn_sched_barrier(0);
      f4 a0 = {0.f, 0.f, 0.f, 0.f};
#pragma unroll
      for (int ks = 0; ks < 8; ++ks) a0 = mfma(wf[ks], bq[ks], a0);
      put(ws, off, enc4(a0, tag));
    }
  } else if (isP0 && !urgent && jr < 21) {
    if (w < 4) {
      const int lt = 21 + jr;
      if (w >= 2) {
        lwait(FL + F_PUB, 2);
        dma_tile<true>(mk(wmod, D * 5376), lt * 128, 5376, (w - 2) * 2, 4, 14, lb + L_TILB + (w - 2) * 2048, 4096, lane);
      }
      unsigned tb = L_TILB + tr_base(lane, w), ao = L_ACT + (n & 7) * AS + g * 16;
      int off = n >= 8 ? 0x7ffff000 : WS_MOD + (n * MODS + lt * 16 + w * 4 + g) * 16;
      v4u wf[14], bq[14];
      lwait(FL + F_ACT, 8);
#pragma unroll
      for (int ks = 0; ks < 14; ++ks) bq[ks] = *(LAS v4u*)(S + ao + ks * 64);
      asm volatile("s_waitcnt vmcnt(0)" ::: "memory");
      lset(FL + F_TILB, w);
      lwait(FL + F_TILB, 4);
#pragma unroll
      for (int ks = 0; ks < 14; ++ks) wf[ks] = trfrag(S + tb + ks * 4096);
      f4 acc = {0.f, 0.f, 0.f, 0.f};
#pragma unroll
      for (int ks = 0; ks < 14; ++ks) acc = mfma(wf[ks], bq[ks], acc);
      put(ws, off, enc4(f4{rbf(acc.x), rbf(acc.y), rbf(acc.z), rbf(acc.w)}, tag));
    }
  } else if (isF) {
    if (w < 4) {
      v8u f1[7], f3[7];
      unsigned tb = L_TILE + tr_base(lane, w), tb3 = tb + L_TILB;
      PIN(tb); PIN(tb3);
      lwait4(FL + F_TILE);
#pragma unroll
      for (int ks = 0; ks < 7; ++ks) f1[ks] = trfrag2(S + tb + ks * 8192);
#pragma unroll
      for (int ks = 0; ks < 7; ++ks) PIN(f1[ks]);
      lset(FL + F_GO, w);
      if (w >= 2) {
        lwait(FL + F_PV, nun);
        dma_tile<true>(mk(w3, D * 3584), fidx * 128, 3584, (w - 2) * 2, 4, 14, lb + L_TILB + (w - 2) * 2048, 4096, lane);
        asm volatile("s_waitcnt vmcnt(0)" ::: "memory");
        lset(FL + F_TILB, w);
      }
      lwait(FL + F_TILB, 2);
#pragma unroll
      for (int ks = 0; ks < 7; ++ks) f3[ks] = trfrag2(S + tb3 + ks * 8192);
      unsigned ao = L_ACT + (n & 7) * AS + (n >> 3) * 448 + g * 16;
      unsigned ho = L_HH + ((g & 1) * 4 + (g & 2)) * HS + (n & 2) * 32 + w * 16 + (n & 12) + (n & 1) * 2;
      int sel = n < 8 ? 0x4444 : 0xEEEE;
      f4 ga = {0.f, 0.f, 0.f, 0.f}, ua = ga;
      PIN(ao); PIN(ho); PIN(sel); PIN(ga); PIN(ua);
      BARRIER();
      v4u bq[7];
#pragma unroll
      for (int ks = 0; ks < 7; ++ks) bq[ks] = *(LAS v4u*)(S + ao + ks * 64);
      __builtin_amdgcn_sched_barrier(0);
#pragma unroll
      for (int ks = 0; ks < 7; ++ks) { ga = mfma2(bq[ks], f1[ks], ga, sel); if (!(ks & 1)) ua = mfma2(bq[ks >> 1], f3[ks >> 1], ua, sel); __builtin_amdgcn_sched_barrier(0); }
      ua = mfma2(bq[4], f3[4], ua, sel);
      __builtin_amdgcn_sched_barrier(0);
      const unsigned gp = pk(halves(ga.x, ga.z), halves(ga.y, ga.w));
      ua = mfma2(bq[5], f3[5], ua, sel);
      ua = mfma2(bq[6], f3[6], ua, sel);
      __builtin_amdgcn_sched_barrier(0);
      const unsigned sp = pk(silu(blo(gp)), silu(bhi(gp))), up = pk(halves(ua.x, ua.z), halves(ua.y, ua.w));
      unsigned hh = pk(blo(sp) * blo(up), bhi(sp) * bhi(up));
      PIN(hh);
      *(LAS unsigned short*)(S + ho) = (unsigned short)hh;
      *(LAS unsigned short*)(S + ho + HS) = (unsigned short)(hh >> 16);
      BARRIER();
    } else {
      const int bb = w - 4;
      v4u m[4];
      poll<4, 896>(m, ws, WS_MOD + (bb * MODS + 336 + L) * 16, tag);
      f4 sh[2], sc[2];
#pragma unroll
      for (int i = 0; i < 8; ++i) { sh[i >> 2][i & 3] = bhi(m[i >> 2][i & 3]); sc[i >> 2][i & 3] = rbf(1.0f + bhi(m[2 + (i >> 2)][i & 3])); }
      PIN(sh[0]); PIN(sh[1]); PIN(sc[0]); PIN(sc[1]);
      lwait4(FL + F_GO);
      const int cs = bb < 7 ? bb : 6;
      if (bb < 7) dma_tile<true>(mk(w2, 1792 * 896), fidx * 57344 + bb * 128, 896, 0, 2, 4, lb + L_TILE + bb * 8192, 2048, lane);
      asm volatile("s_waitcnt vmcnt(0)" ::: "memory");
      v8u f2_[4];
#pragma unroll
      for (int i = 0; i < 4; ++i) f2_[i] = trfrag2(S + L_TILE + cs * 8192 + tr_base(lane, i));
#pragma unroll
      for (int i = 0; i < 4; ++i) PIN(f2_[i]);
      unsigned ao = L_ACT + bb * AS + L * 4, ho = L_HH + (n & 7) * HS + (n >> 3) * 64 + g * 16;
      int sel = n < 8 ? 0x4444 : 0xEEEE;
      f4 d[4];
#pragma unroll
      for (int i = 0; i < 4; ++i) { d[i] = f4{0.f, 0.f, 0.f, 0.f}; PIN(d[i]); }
      PIN(sel);
      float cnt = act ? 1.0f / D : 0.f;
      int off = WS_S3P + (((g & 1) * 28 + bb * 4 + (g >> 1)) * S3S + fidx * 16 + n) * 16;
      PIN(ao); PIN(ho); PIN(off); PIN(cnt);
      lwait(FL + F_TILB, 2);
      v4u xg[2];
      poll<2, 896>(xg, ws, WS_X1 + (bb * 112 + L) * 16, tag);
      f4 v0 = {bhi(xg[0].x), bhi(xg[0].y), bhi(xg[0].z), bhi(xg[0].w)}, v1 = {bhi(xg[1].x), bhi(xg[1].y), bhi(xg[1].z), bhi(xg[1].w)};
      float nx = asf(__builtin_amdgcn_readfirstlane(asu(v0.x)) ^ 0x80000000u);
      PIN(nx);
      v0 += nx; v1 += nx;
      const f4 t1 = v0 + v1, t2 = v0 * v0 + v1 * v1;
      const f2 u1 = t1.xy + t1.zw, u2 = t2.xy + t2.zw;
      float mo = halves((u1.x + u1.y) * cnt, (u2.x + u2.y) * cnt);
      mo += dpp<XOR1>(mo);
      v0 *= sc[0]; asm("" : "+v"(v0), "+v"(mo));
      mo += dpp<XOR2>(mo);
      v1 *= sc[1]; asm("" : "+v"(v1), "+v"(mo));
      mo += dpp<ROR4>(mo); mo += dpp<ROR8>(mo); mo += dpp<0x142>(mo);
      const float m1 = asf(__builtin_amdgcn_readlane(asu(mo), 16)), m2 = asf(__builtin_amdgcn_readlane(asu(mo), 48));
      const float var = rbf(fmaxf(__builtin_fmaf(-m1, m1, m2), 0.f)), dm = -nx - rbf(m1 - nx);
      float sd = __builtin_amdgcn_sqrtf(rbf(var + 1e-5f));
      v0 += dm * sc[0]; v1 += dm * sc[1];
      asm("" : "+v"(v0), "+v"(sd));
      const float inv = __builtin_amdgcn_rcpf(rbf(sd));
      v0 = v0 * inv + sh[0]; v1 = v1 * inv + sh[1];
      *(LAS unsigned*)(S + ao) = pk(v0.x, v0.y);
      *(LAS unsigned*)(S + ao + 448) = pk(v0.z, v0.w);
      *(LAS unsigned*)(S + ao + 224) = pk(v1.x, v1.y);
      *(LAS unsigned*)(S + ao + 672) = pk(v1.z, v1.w);
      asm volatile("s_waitcnt lgkmcnt(0)\n\ts_barrier\n\ts_barrier" ::: "memory");
      if (bb < 7) {
        const v4u h0 = *(LAS v4u*)(S + ho);
#pragma unroll
        for (int cc = 0; cc < 4; ++cc) d[cc] = mfma2(h0, f2_[cc], d[cc], sel);
        put(ws, off, enc4(halves4(d[0], d[1]), tag));
        put(ws, off + 2 * S3S * 16, enc4(halves4(d[2], d[3]), tag));
      }
    }
  } else if (j == 1 && x < 7 && w == 4) {
      const unsigned xv = __builtin_amdgcn_raw_buffer_load_b16(mk(xin, 8 * D * 2), b * 896 + x * 128 + n * 8 + g * 2, 0, 0);
      lwait(FL + F_PUB, 2);
      v4u gg[1];
      poll<1, 0>(gg, ws, WS_MOD + (b * MODS + 224 + x * 16 + n) * 16, tag);
      float xf = blo(xv), gf = bhi(g & 2 ? (g & 1 ? gg[0].w : gg[0].z) : (g & 1 ? gg[0].y : gg[0].x));
      int o1 = lane < 16 ? WS_X1 + (b * 112 + x * 16 + n) * 16 : 0x7ffffff0;
      unsigned sel = 0x03020104u + g;
      PIN(xf); PIN(gf); PIN(o1); PIN(sel);
      v4u p1[4];
      poll<4, 1024>(p1, ws, WS_S1P + (b * S1S + x * 256 + lane) * 16, tag);
      const float x1 = rbf(xf + rbf(gf * rbf(scatter_sum((dec4(p1[0]) + dec4(p1[1])) + (dec4(p1[2]) + dec4(p1[3]))))));
      const unsigned e = __builtin_amdgcn_perm(tag, asu(x1), sel);
      const auto s = __builtin_amdgcn_permlane16_swap(e, e, false, false);
      const auto t = __builtin_amdgcn_permlane32_swap(s[0], s[0], false, false), u = __builtin_amdgcn_permlane32_swap(s[1], s[1], false, false);
      put(ws, o1, v4u{t[0], u[0], t[1], u[1]});
  } else if (j == 1 && x < 7 && w == 5) {
      const int rid = (b >> 2) * 28 + (b & 3) * 7 + x, rb = (b & 4) + g, rc = ((b & 3) * 7 + x) * 16 + n;
      int o3 = WS_S3P + (rid * S3S + lane) * 16, oo = rb * 896 + rc * 2;
      lwait(FL + F_PUB, 2);
      touch(mk(out, 8 * D * 2), oo);
      __builtin_amdgcn_s_sleep(RSLP);
      v4u gg[1];
      poll<1, 0>(gg, ws, WS_MOD + (rb * MODS + 560 + (rc >> 2)) * 16, tag);
      float gf = bhi(n & 2 ? (n & 1 ? gg[0].w : gg[0].z) : (n & 1 ? gg[0].y : gg[0].x));
      poll<1, 0, 8>(gg, ws, WS_X1 + (rb * 112 + (rc >> 2)) * 16, tag);
      float x1 = bhi(n & 2 ? (n & 1 ? gg[0].w : gg[0].z) : (n & 1 ? gg[0].y : gg[0].x));
      PIN(gf); PIN(x1); PIN(o3); PIN(oo);
      v4u p3[7];
      poll<7, 1024>(p3, ws, o3, tag);
      const float y = x1 + rbf(gf * rbf(scatter_sum(((dec4(p3[0]) + dec4(p3[1])) + (dec4(p3[2]) + dec4(p3[3]))) + ((dec4(p3[4]) + dec4(p3[5])) + dec4(p3[6])))));
      __builtin_amdgcn_raw_buffer_store_b16((unsigned short)pk(y, y), mk(out, 8 * D * 2), oo, 0, 16);
      if (idx == 0 && lane == 0) __builtin_amdgcn_raw_buffer_store_b32(tag, ws, WS_EPOCH, 0, 16);
  }
}

#undef kc
#undef vc
#undef ws_
#undef wmod
#undef wq
#undef cin
#undef xin
#undef wo
#undef w1
#undef w3
#undef w2
#undef out
#undef tag

extern "C" void kernel_launch(void* const* d_in, const int* in_sizes, int n_in, void* d_out, int out_size, void* d_ws, size_t ws_size,
                              hipStream_t stream) {
  fused_decode<<<256, 768, 0, stream>>>((const char*)d_in[1], (const char*)d_in[2], (char*)d_ws, (const char*)d_in[10], (const char*)d_in[3],
                                        (const char*)d_in[9], (const char*)d_in[0], (const char*)d_in[4], (const char*)d_in[6],
                                        (const char*)d_in[7], (const char*)d_in[8], (char*)d_out);
}
```
